```python
import torch

def _softmax(x, axis=-1):
    return torch.softmax(x, dim=axis)
def _gelu(x):
    return torch.nn.functional.gelu(x, approximate="tanh")
def _silu(x):
    return torch.nn.functional.silu(x)
def _sigmoid(x):
    return torch.sigmoid(x)
def _layernorm(x, axis=-1, eps=1e-5):
    m = x.mean(dim=axis, keepdim=True)
    v = x.var(dim=axis, keepdim=True, unbiased=False)
    return (x - m) / torch.sqrt(v + eps)
def _scatter_add(tgt, idx, src, axis=0):
    return tgt.clone().index_add_(axis, idx, src)
def _segment_sum(src, seg, n):
    out = torch.zeros((n,) + src.shape[1:], dtype=src.dtype)
    return out.index_add_(0, seg, src)
def _pad(x, pw):
    flat = tuple(v for pair in reversed(pw) for v in pair)
    return torch.nn.functional.pad(x, flat)

kernel_name = 'recurrent_scan__gqa_block_cached__geglu_ffn__decode'

def setup_inputs():

    _w = (lambda a: (lambda t: t.to(torch.bfloat16) if t.is_floating_point() else t)(torch.as_tensor(a)))
    def _A_setup():
        B, Sq, D = 2, 1, 512
        Hq, Hkv, Sk, Dh = 32, 32, 8192, 128
        assert Hq % Hkv == 0, (Hq, Hkv)
        return {
            "x": _w((lambda *s: torch.randn(*s, dtype=torch.bfloat16) * 0.05)(B, Sq, D)),
            "k_cache": _w((lambda *s: torch.randn(*s, dtype=torch.bfloat16) * 1.6)(B, Hkv, Sk, Dh)),
            "v_cache": _w((lambda *s: torch.randn(*s, dtype=torch.bfloat16) * 0.2)(B, Hkv, Sk, Dh)),
            "wq": _w((lambda *s: torch.randn(*s, dtype=torch.bfloat16) * 1.6)(D, Hq * Dh) * (D ** -0.5)),
            "wo": _w((lambda *s: torch.randn(*s, dtype=torch.bfloat16) * 0.2)(Hq * Dh, D) * ((Hq * Dh) ** -0.5)),
        }
    _A_in = _A_setup()

    def _B_setup():
        B, S, D, F = 2, 1, 512, 1376
        return {
            "x": _w((lambda *s: torch.randn(*s, dtype=torch.bfloat16) * 0.05)(B, S, D)),
            "w_gate": _w((lambda *s: torch.randn(*s, dtype=torch.bfloat16) * 0.1)(D, F)),
            "w_up": _w((lambda *s: torch.randn(*s, dtype=torch.bfloat16) * 0.1)(D, F)),
            "w_down": _w((lambda *s: torch.randn(*s, dtype=torch.bfloat16) * 0.1)(F, D)),
        }
    _B_in = _B_setup()

    return {
        "A_x": _A_in["x"],
        "A_k_cache": _A_in["k_cache"],
        "A_v_cache": _A_in["v_cache"],
        "A_wq": _A_in["wq"],
        "A_wo": _A_in["wo"],
        "B_x": _B_in["x"],
        "B_w_gate": _B_in["w_gate"],
        "B_w_up": _B_in["w_up"],
        "B_w_down": _B_in["w_down"],
    }

def reference(A_x, A_k_cache, A_v_cache, A_wq, A_wo, B_x, B_w_gate, B_w_up, B_w_down):
    def _A_ref(x, k_cache, v_cache, wq, wo):
        B, Sq, D = x.shape
        Hq, Dh = 32, 128
        Hkv = k_cache.shape[1]
        rep = Hq // Hkv
        q = (lambda x, *d: x.permute(*d))((x @ wq).reshape(B, Sq, Hq, Dh), 0, 2, 1, 3)
        k = (lambda x, n, axis: torch.repeat_interleave(x, n, dim=axis))(k_cache, rep, axis=1)
        v = (lambda x, n, axis: torch.repeat_interleave(x, n, dim=axis))(v_cache, rep, axis=1)
        scores = torch.einsum("bhqd,bhkd->bhqk", q, k) * (Dh ** -0.5)
        p = _softmax(scores, axis=-1)
        o = torch.einsum("bhqk,bhkd->bhqd", p, v)
        return (lambda x, *d: x.permute(*d))(o, 0, 2, 1, 3).reshape(B, Sq, Hq * Dh) @ wo

    def _B_ref(x, w_gate, w_up, w_down):
        return (_gelu(x @ w_gate) * (x @ w_up)) @ w_down

    x = A_x
    xn = _layernorm(x, axis=-1)
    s = _A_ref(xn, A_k_cache, A_v_cache, A_wq, A_wo)
    g = _silu(_B_ref(xn, B_w_gate, B_w_up, B_w_down))
    return x + s * g
```

```cpp
#include <hip/hip_runtime.h>
#include <hip/hip_bf16.h>
#include <stdint.h>

using u32x4 = __attribute__((ext_vector_type(4))) unsigned;
using f32x4 = __attribute__((ext_vector_type(4))) float;
using bf16x2 = __attribute__((ext_vector_type(2))) __bf16;
typedef unsigned short u16;

constexpr int DM = 512, HQ = 32, SK = 8192, DH = 128, FF = 1376;
constexpr int NSPLIT = 4, KPB = SK / NSPLIT;
constexpr int NWAVE = 8, G = 8;
constexpr int NGROUP = KPB / 4 / G / NWAVE;
constexpr int NUNIT = FF / 8;
constexpr int NSLOT = HQ * NSPLIT;
constexpr float LOG2E = 1.4426950408889634f;
constexpr float SCALE = 0.08838834764831845f;

constexpr size_t WS_Y = 0;
constexpr size_t WS_ML = WS_Y + (size_t)2 * NSLOT * DM * 4;
constexpr size_t WS_F = WS_ML + (size_t)2 * NSLOT * 2 * 4;

__device__ __forceinline__ float dot2(unsigned a, unsigned b, float acc) {
  return __builtin_amdgcn_fdot2_f32_bf16(__builtin_bit_cast(bf16x2, a), __builtin_bit_cast(bf16x2, b), acc, false);
}
template <int CTRL> __device__ __forceinline__ float dpp(float x) {
  return __builtin_bit_cast(float, __builtin_amdgcn_mov_dpp(__builtin_bit_cast(int, x), CTRL, 0xf, 0xf, true));
}
constexpr int ROR8 = 0x128, ROR4 = 0x124, ROR2 = 0x122, ROR1 = 0x121, XOR2 = 0x4E;
__device__ __forceinline__ float row16_sum(float x) {
  x += dpp<ROR8>(x); x += dpp<ROR4>(x); x += dpp<ROR2>(x); x += dpp<ROR1>(x); return x;
}
__device__ __forceinline__ float xrow_sum(float x) {
  auto s = __builtin_amdgcn_permlane16_swap(__float_as_uint(x), __float_as_uint(x), false, false);
  x = __uint_as_float(s[0]) + __uint_as_float(s[1]);
  auto t = __builtin_amdgcn_permlane32_swap(__float_as_uint(x), __float_as_uint(x), false, false);
  return __uint_as_float(t[0]) + __uint_as_float(t[1]);
}
__device__ __forceinline__ float xrow_max(float x) {
  auto s = __builtin_amdgcn_permlane16_swap(__float_as_uint(x), __float_as_uint(x), false, false);
  x = fmaxf(__uint_as_float(s[0]), __uint_as_float(s[1]));
  auto t = __builtin_amdgcn_permlane32_swap(__float_as_uint(x), __float_as_uint(x), false, false);
  return fmaxf(__uint_as_float(t[0]), __uint_as_float(t[1]));
}
__device__ __forceinline__ float wave_sum(float x) { return xrow_sum(row16_sum(x)); }
__device__ __forceinline__ float rbf(float x) {
  return (float)(__bf16)x;
}
__device__ __forceinline__ float bflo(unsigned v) { return __uint_as_float(v << 16); }
__device__ __forceinline__ float bfhi(unsigned v) { return __uint_as_float(v & 0xffff0000u); }
__device__ __forceinline__ unsigned packbf(float lo, float hi) {
  bf16x2 r = {(__bf16)lo, (__bf16)hi};
  return __builtin_bit_cast(unsigned, r);
}
__device__ __forceinline__ u32x4 ldnt(const u32x4* p) { return __builtin_nontemporal_load(p); }

__device__ __forceinline__ void layernorm8(u32x4 xv, float (&xn)[8]) {
  float xf[8];
#pragma unroll
  for (int k = 0; k < 4; ++k) { xf[2 * k] = bflo(xv[k]); xf[2 * k + 1] = bfhi(xv[k]); }
  float s = 0.f;
#pragma unroll
  for (int k = 0; k < 8; ++k) s += xf[k];
  const float mean = wave_sum(s) * (1.f / DM);
  float q = 0.f;
#pragma unroll
  for (int k = 0; k < 8; ++k) { const float d = xf[k] - mean; q = fmaf(d, d, q); }
  const float var = wave_sum(q) * (1.f / DM);
  const float mb = rbf(mean), vb = rbf(var);
  const float den = rbf(sqrtf(rbf(vb + 1e-5f)));
#pragma unroll
  for (int k = 0; k < 8; ++k) xn[k] = rbf(rbf(xf[k] - mb) / den);
}
__device__ __forceinline__ float gelu_tanh(float x) {
  const float inner = 0.7978845608028654f * (x + 0.044715f * x * x * x);
  return 0.5f * x * (1.f + tanhf(inner));
}

constexpr int L_XN = 0;
constexpr int L_QP = L_XN + NWAVE * 128;
constexpr int L_FP = L_QP + NWAVE * 128;
constexpr int L_OW = L_FP + NWAVE * 32;
constexpr int L_ML = L_OW + NWAVE * 128;
constexpr int L_YW = L_ML + NWAVE * 2;
constexpr int L_END = L_YW + NWAVE * 512;

__global__ void __launch_bounds__(512, 2)
k_main(const u16* __restrict__ x, const u16* __restrict__ kc, const u16* __restrict__ vc, const u16* __restrict__ wq,
       const u16* __restrict__ wo, const u16* __restrict__ wg, const u16* __restrict__ wu, const u16* __restrict__ wd,
       unsigned char* __restrict__ ws) {
  __shared__ __attribute__((aligned(16))) float lds[L_END];
  const int tid = threadIdx.x, lane = tid & 63, wave = __builtin_amdgcn_readfirstlane(tid >> 6);
  const int r = lane >> 4, c = lane & 15;
  const int bid = blockIdx.x, g8 = bid & 7, i = bid >> 3;
  const int h = g8 * 4 + (i >> 3), b = (i >> 2) & 1, s = i & 3;
  const int uq = g8 + 8 * (i >> 2);
  const bool has_ffn = uq < NUNIT / 4;
  const int unit = has_ffn ? uq * 4 + (i & 3) : 0;

  const u32x4 xv0 = *reinterpret_cast<const u32x4*>(x + 8 * lane);
  const u32x4 xv1 = *reinterpret_cast<const u32x4*>(x + DM + 8 * lane);
  u32x4 wqv[16];
  {
    const u16* p = wq + (size_t)(wave * 64 + r) * (HQ * DH) + h * DH + 8 * c;
#pragma unroll
    for (int t = 0; t < 16; ++t) wqv[t] = *reinterpret_cast<const u32x4*>(p + (size_t)t * 4 * (HQ * DH));
  }
  u32x4 wgv, wuv, wdv;
  if (has_ffn) {
    wgv = *reinterpret_cast<const u32x4*>(wg + (size_t)(wave * 64 + lane) * FF + unit * 8);
    wuv = *reinterpret_cast<const u32x4*>(wu + (size_t)(wave * 64 + lane) * FF + unit * 8);
    wdv = *reinterpret_cast<const u32x4*>(wd + (size_t)(unit * 8 + (lane >> 3)) * DM + wave * 64 + 8 * (lane & 7));
  }
  const size_t kvoff = ((size_t)(b * HQ + h) * SK + (size_t)s * KPB) * DH;
  const u32x4* kp = reinterpret_cast<const u32x4*>(kc + kvoff) + wave * G * 64 + lane;
  const u32x4* vp = reinterpret_cast<const u32x4*>(vc + kvoff) + wave * G * 64 + lane;
  constexpr int GSTR = NWAVE * G * 64;
  u32x4 Ka[G], Va[G], Kb[G], Vb[G];
#pragma unroll
  for (int t = 0; t < G; ++t) Ka[t] = ldnt(kp + t * 64);
#pragma unroll
  for (int t = 0; t < G; ++t) Va[t] = ldnt(vp + t * 64);
#pragma unroll
  for (int t = 0; t < G; ++t) Kb[t] = ldnt(kp + GSTR + t * 64);
#pragma unroll
  for (int t = 0; t < G; ++t) Vb[t] = ldnt(vp + GSTR + t * 64);
  __builtin_amdgcn_sched_barrier(0);

  {
    float xn0[8], xn1[8];
    layernorm8(xv0, xn0);
    layernorm8(xv1, xn1);
    if ((lane >> 3) == wave) {
      float* p = lds + L_XN + wave * 128 + (lane & 7) * 8;
      *reinterpret_cast<f32x4*>(p) = f32x4{xn0[0], xn0[1], xn0[2], xn0[3]};
      *reinterpret_cast<f32x4*>(p + 4) = f32x4{xn0[4], xn0[5], xn0[6], xn0[7]};
      *reinterpret_cast<f32x4*>(p + 64) = f32x4{xn1[0], xn1[1], xn1[2], xn1[3]};
      *reinterpret_cast<f32x4*>(p + 68) = f32x4{xn1[4], xn1[5], xn1[6], xn1[7]};
    }
  }
  __builtin_amdgcn_fence(__ATOMIC_RELEASE, "wavefront");
  __builtin_amdgcn_wave_barrier();
  __builtin_amdgcn_fence(__ATOMIC_ACQUIRE, "wavefront");
  {
    float qa[8];
#pragma unroll
    for (int k = 0; k < 8; ++k) qa[k] = 0.f;
    const float* xr = lds + L_XN + wave * 128 + b * 64 + r;
#pragma unroll
    for (int t = 0; t < 16; ++t) {
      const float xe = xr[4 * t];
#pragma unroll
      for (int k = 0; k < 4; ++k) {
        qa[2 * k] = fmaf(xe, bflo(wqv[t][k]), qa[2 * k]);
        qa[2 * k + 1] = fmaf(xe, bfhi(wqv[t][k]), qa[2 * k + 1]);
      }
    }
#pragma unroll
    for (int k = 0; k < 8; ++k) qa[k] = xrow_sum(qa[k]);
    if (r == 0) {
      float* p = lds + L_QP + wave * 128 + 8 * c;
      *reinterpret_cast<f32x4*>(p) = f32x4{qa[0], qa[1], qa[2], qa[3]};
      *reinterpret_cast<f32x4*>(p + 4) = f32x4{qa[4], qa[5], qa[6], qa[7]};
    }
  }
  if (has_ffn) {
    const float x0 = lds[L_XN + wave * 128 + lane], x1 = lds[L_XN + wave * 128 + 64 + lane];
    float f[32];
#pragma unroll
    for (int k = 0; k < 4; ++k) {
      const float g0 = bflo(wgv[k]), g1 = bfhi(wgv[k]), u0 = bflo(wuv[k]), u1 = bfhi(wuv[k]);
      f[2 * k] = x0 * g0; f[2 * k + 1] = x0 * g1; f[8 + 2 * k] = x1 * g0; f[8 + 2 * k + 1] = x1 * g1;
      f[16 + 2 * k] = x0 * u0; f[16 + 2 * k + 1] = x0 * u1; f[24 + 2 * k] = x1 * u0; f[24 + 2 * k + 1] = x1 * u1;
    }
#pragma unroll
    for (int k = 0; k < 32; ++k) f[k] = wave_sum(f[k]);
    if (lane == 0) {
      float* p = lds + L_FP + wave * 32;
#pragma unroll
      for (int k = 0; k < 32; k += 4) *reinterpret_cast<f32x4*>(p + k) = f32x4{f[k], f[k + 1], f[k + 2], f[k + 3]};
    }
  }
  __syncthreads();
  unsigned qpk[4];
  {
    f32x4 a0 = {0.f, 0.f, 0.f, 0.f}, a1 = {0.f, 0.f, 0.f, 0.f};
#pragma unroll
    for (int w = 0; w < NWAVE; ++w) {
      a0 += *reinterpret_cast<const f32x4*>(lds + L_QP + w * 128 + 8 * c);
      a1 += *reinterpret_cast<const f32x4*>(lds + L_QP + w * 128 + 8 * c + 4);
    }
    qpk[0] = packbf(a0[0], a0[1]); qpk[1] = packbf(a0[2], a0[3]);
    qpk[2] = packbf(a1[0], a1[1]); qpk[3] = packbf(a1[2], a1[3]);
  }
  if (has_ffn) {
    const int rr = lane >> 3;
    float g0 = 0.f, g1 = 0.f, u0 = 0.f, u1 = 0.f;
#pragma unroll
    for (int w = 0; w < NWAVE; ++w) {
      const float* p = lds + L_FP + w * 32 + rr;
      g0 += p[0]; g1 += p[8]; u0 += p[16]; u1 += p[24];
    }
    const float h0 = rbf(rbf(gelu_tanh(rbf(g0))) * rbf(u0));
    const float h1 = rbf(rbf(gelu_tanh(rbf(g1))) * rbf(u1));
    float o0[8], o1[8];
#pragma unroll
    for (int k = 0; k < 4; ++k) {
      const float d0 = bflo(wdv[k]), d1 = bfhi(wdv[k]);
      o0[2 * k] = h0 * d0; o0[2 * k + 1] = h0 * d1; o1[2 * k] = h1 * d0; o1[2 * k + 1] = h1 * d1;
    }
#pragma unroll
    for (int k = 0; k < 8; ++k) {
      o0[k] += dpp<ROR8>(o0[k]); o0[k] = xrow_sum(o0[k]);
      o1[k] += dpp<ROR8>(o1[k]); o1[k] = xrow_sum(o1[k]);
    }
    if (lane < 8) {
      float* fp = reinterpret_cast<float*>(ws + WS_F) + (size_t)unit * DM + wave * 64 + 8 * lane;
      *reinterpret_cast<f32x4*>(fp) = f32x4{o0[0], o0[1], o0[2], o0[3]};
      *reinterpret_cast<f32x4*>(fp + 4) = f32x4{o0[4], o0[5], o0[6], o0[7]};
      fp += (size_t)NUNIT * DM;
      *reinterpret_cast<f32x4*>(fp) = f32x4{o1[0], o1[1], o1[2], o1[3]};
      *reinterpret_cast<f32x4*>(fp + 4) = f32x4{o1[4], o1[5], o1[6], o1[7]};
    }
  }

  float m = -INFINITY, l = 0.f, acc[8];
#pragma unroll
  for (int k = 0; k < 8; ++k) acc[k] = 0.f;
  const u32x4* wop = reinterpret_cast<const u32x4*>(wo + (size_t)(h * DH + wave * 16) * DM) + lane;

#define PROCESS(KR, VR, MODE, gn)                                                        \
  {                                                                                      \
    float sc[G];                                                                         \
    _Pragma("unroll") for (int t = 0; t < G; ++t) {                                      \
      float d = dot2(KR[t][0], qpk[0], 0.f);                                             \
      d = dot2(KR[t][1], qpk[1], d); d = dot2(KR[t][2], qpk[2], d); d = dot2(KR[t][3], qpk[3], d); \
      d = row16_sum(d);                                                                  \
      sc[t] = rbf(rbf(d) * SCALE) * LOG2E;                                               \
    }                                                                                    \
    __builtin_amdgcn_sched_barrier(0);                                                   \
    if (MODE == 0) { _Pragma("unroll") for (int t = 0; t < G; ++t) KR[t] = ldnt(kp + (size_t)(gn) * GSTR + t * 64); } \
    if (MODE == 1) { _Pragma("unroll") for (int t = 0; t < G; ++t) KR[t] = wop[t * 64]; } \
    __builtin_amdgcn_sched_barrier(0);                                                   \
    float mx = m;                                                                        \
    _Pragma("unroll") for (int t = 0; t < G; ++t) mx = fmaxf(mx, sc[t]);                 \
    const float alpha = __builtin_amdgcn_exp2f(m - mx);                                  \
    m = mx; l *= alpha;                                                                  \
    _Pragma("unroll") for (int k = 0; k < 8; ++k) acc[k] *= alpha;                       \
    _Pragma("unroll") for (int t = 0; t < G; ++t) {                                      \
      const float p = __builtin_amdgcn_exp2f(sc[t] - mx);                                \
      l += p;                                                                            \
      _Pragma("unroll") for (int k = 0; k < 4; ++k) {                                    \
        acc[2 * k] = fmaf(p, bflo(VR[t][k]), acc[2 * k]);                                \
        acc[2 * k + 1] = fmaf(p, bfhi(VR[t][k]), acc[2 * k + 1]);                        \
      }                                                                                  \
    }                                                                                    \
    __builtin_amdgcn_sched_barrier(0);                                                   \
    if (MODE == 0) { _Pragma("unroll") for (int t = 0; t < G; ++t) VR[t] = ldnt(vp + (size_t)(gn) * GSTR + t * 64); } \
    if (MODE == 1) { _Pragma("unroll") for (int t = 0; t < G; ++t) VR[t] = wop[(G + t) * 64]; } \
    __builtin_amdgcn_sched_barrier(0);                                                   \
  }

  for (int g = 0; g < NGROUP - 2; g += 2) {
    PROCESS(Ka, Va, 0, g + 2)
    PROCESS(Kb, Vb, 0, g + 3)
  }
  PROCESS(Ka, Va, 1, 0)
  PROCESS(Kb, Vb, 2, 0)
#undef PROCESS

  {
    const float M = xrow_max(m);
    const float sc = __builtin_amdgcn_exp2f(m - M);
    l = xrow_sum(l * sc);
#pragma unroll
    for (int k = 0; k < 8; ++k) acc[k] = xrow_sum(acc[k] * sc);
    if (r == 0) {
      float* p = lds + L_OW + wave * 128 + 8 * c;
      *reinterpret_cast<f32x4*>(p) = f32x4{acc[0], acc[1], acc[2], acc[3]};
      *reinterpret_cast<f32x4*>(p + 4) = f32x4{acc[4], acc[5], acc[6], acc[7]};
      if (c == 0) { lds[L_ML + wave * 2] = M; lds[L_ML + wave * 2 + 1] = l; }
    }
  }
  __syncthreads();
  float Mb = -INFINITY, Lb = 0.f;
  {
    float mw[NWAVE], cw[NWAVE];
#pragma unroll
    for (int w = 0; w < NWAVE; ++w) { mw[w] = lds[L_ML + 2 * w]; Mb = fmaxf(Mb, mw[w]); }
    float o = 0.f;
#pragma unroll
    for (int w = 0; w < NWAVE; ++w) {
      cw[w] = __builtin_amdgcn_exp2f(mw[w] - Mb);
      Lb = fmaf(cw[w], lds[L_ML + 2 * w + 1], Lb);
      o = fmaf(cw[w], lds[L_OW + w * 128 + wave * 16 + c], o);
    }
    float y[8];
#pragma unroll
    for (int k = 0; k < 8; ++k) y[k] = 0.f;
#pragma unroll
    for (int t = 0; t < 16; ++t) {
      const float ot = __builtin_bit_cast(float, __builtin_amdgcn_readlane(__builtin_bit_cast(int, o), t));
      const u32x4 wv = t < G ? Ka[t] : Va[t - G];
#pragma unroll
      for (int k = 0; k < 4; ++k) {
        y[2 * k] = fmaf(ot, bflo(wv[k]), y[2 * k]);
        y[2 * k + 1] = fmaf(ot, bfhi(wv[k]), y[2 * k + 1]);
      }
    }
    float* p = lds + L_YW + wave * 512 + 8 * lane;
    *reinterpret_cast<f32x4*>(p) = f32x4{y[0], y[1], y[2], y[3]};
    *reinterpret_cast<f32x4*>(p + 4) = f32x4{y[4], y[5], y[6], y[7]};
  }
  __syncthreads();
  {
    float y = 0.f;
#pragma unroll
    for (int w = 0; w < NWAVE; ++w) y += lds[L_YW + w * 512 + tid];
    const int slot = b * NSLOT + h * NSPLIT + s;
    reinterpret_cast<float*>(ws + WS_Y)[(size_t)slot * DM + tid] = y;
    if (tid == 0) *reinterpret_cast<float2*>(ws + WS_ML + (size_t)slot * 8) = make_float2(Mb, Lb);
  }
}

__global__ void __launch_bounds__(512)
k_final(const u16* __restrict__ x, const unsigned char* __restrict__ ws, u16* __restrict__ out) {
  __shared__ __attribute__((aligned(16))) float red[NWAVE][2][8];
  const int tid = threadIdx.x, lane = tid & 63, wave = tid >> 6;
  const int b = blockIdx.x & 1, cg = blockIdx.x >> 1;
  const int row = tid >> 1, half = tid & 1;
  f32x4 sa = {0.f, 0.f, 0.f, 0.f}, sf = {0.f, 0.f, 0.f, 0.f};
  u16 xo = 0;
  if (tid < 8) xo = x[b * DM + cg * 8 + tid];
  if (row < NUNIT) sf = *reinterpret_cast<const f32x4*>(ws + WS_F + (((size_t)b * NUNIT + row) * DM + cg * 8 + half * 4) * 4);
  if (row < NSLOT) {
    const f32x4 y = *reinterpret_cast<const f32x4*>(ws + WS_Y + (((size_t)b * NSLOT + row) * DM + cg * 8 + half * 4) * 4);
    const f32x4* mlp = reinterpret_cast<const f32x4*>(ws + WS_ML + ((size_t)b * NSLOT + (row & ~3)) * 8);
    const f32x4 a = mlp[0], c4 = mlp[1];
    const float M = fmaxf(fmaxf(a[0], a[2]), fmaxf(c4[0], c4[2]));
    const float e0 = __builtin_amdgcn_exp2f(a[0] - M), e1 = __builtin_amdgcn_exp2f(a[2] - M);
    const float e2 = __builtin_amdgcn_exp2f(c4[0] - M), e3 = __builtin_amdgcn_exp2f(c4[2] - M);
    const float L = fmaf(e0, a[1], fmaf(e1, a[3], fmaf(e2, c4[1], e3 * c4[3])));
    const int sp = row & 3;
    const float e = sp == 0 ? e0 : sp == 1 ? e1 : sp == 2 ? e2 : e3;
    sa = y * (e / L);
  }
  float v[8] = {sa[0], sa[1], sa[2], sa[3], sf[0], sf[1], sf[2], sf[3]};
#pragma unroll
  for (int k = 0; k < 8; ++k) {
    float t = v[k];
    t += dpp<XOR2>(t); t += dpp<ROR4>(t); t += dpp<ROR8>(t);
    v[k] = xrow_sum(t);
  }
  if (lane < 2) {
    *reinterpret_cast<f32x4*>(&red[wave][lane][0]) = f32x4{v[0], v[1], v[2], v[3]};
    *reinterpret_cast<f32x4*>(&red[wave][lane][4]) = f32x4{v[4], v[5], v[6], v[7]};
  }
  __syncthreads();
  if (tid < 8) {
    float s = 0.f, f = 0.f;
#pragma unroll
    for (int w = 0; w < NWAVE; ++w) { s += red[w][tid >> 2][tid & 3]; f += red[w][tid >> 2][4 + (tid & 3)]; }
    s = rbf(s); f = rbf(f);
    const float g = rbf(f / (1.f + __expf(-f)));
    const float o = __uint_as_float((unsigned)xo << 16) + rbf(s * g);
    out[b * DM + cg * 8 + tid] = __builtin_bit_cast(u16, (__bf16)o);
  }
}

extern "C" void kernel_launch(void* const* d_in, const int* in_sizes, int n_in, void* d_out, int out_size, void* d_ws,
                              size_t ws_size, hipStream_t stream) {
  const u16* x = (const u16*)d_in[0];
  const u16* kc = (const u16*)d_in[1];
  const u16* vc = (const u16*)d_in[2];
  const u16* wq = (const u16*)d_in[3];
  const u16* wo = (const u16*)d_in[4];
  const u16* wg = (const u16*)d_in[6];
  const u16* wu = (const u16*)d_in[7];
  const u16* wd = (const u16*)d_in[8];
  k_main<<<256, 512, 0, stream>>>(x, kc, vc, wq, wo, wg, wu, wd, (unsigned char*)d_ws);
  k_final<<<128, 512, 0, stream>>>(x, (const unsigned char*)d_ws, (u16*)d_out);
}
```

```cpp
#include <hip/hip_runtime.h>
#include <hip/hip_bf16.h>
#include <stdint.h>

using u32x4 = __attribute__((ext_vector_type(4))) unsigned;
using f32x4 = __attribute__((ext_vector_type(4))) float;
using bf16x2 = __attribute__((ext_vector_type(2))) __bf16;
using bf16x8 = __attribute__((ext_vector_type(8))) __bf16;
typedef unsigned short u16;

constexpr int DM = 512, HQ = 32, SK = 8192, DH = 128, FF = 1376;
constexpr int NSPLIT = 4, KPB = SK / NSPLIT;
constexpr int NWAVE = 8;
constexpr int NGROUP = KPB / 32 / NWAVE;
constexpr int NUNIT = FF / 8;
constexpr int NSLOT = HQ * NSPLIT;
constexpr float LOG2E = 1.4426950408889634f;
constexpr float SCALE = 0.08838834764831845f;

constexpr size_t WS_Y = 0;
constexpr size_t WS_ML = WS_Y + (size_t)2 * NSLOT * DM * 4;
constexpr size_t WS_F = WS_ML + (size_t)2 * NSLOT * 2 * 4;

__device__ __forceinline__ float dot2(unsigned a, unsigned b, float acc) {
  return __builtin_amdgcn_fdot2_f32_bf16(__builtin_bit_cast(bf16x2, a), __builtin_bit_cast(bf16x2, b), acc, false);
}
template <int CTRL> __device__ __forceinline__ float dpp(float x) {
  return __builtin_bit_cast(float, __builtin_amdgcn_mov_dpp(__builtin_bit_cast(int, x), CTRL, 0xf, 0xf, true));
}
template <int CTRL> __device__ __forceinline__ unsigned dppu(unsigned x) {
  return (unsigned)__builtin_amdgcn_mov_dpp((int)x, CTRL, 0xf, 0xf, true);
}
constexpr int ROR8 = 0x128, ROR4 = 0x124, ROR2 = 0x122, ROR1 = 0x121, XOR1 = 0xB1, XOR2 = 0x4E, HMIR = 0x141;
__device__ __forceinline__ float row16_sum(float x) {
  x += dpp<ROR8>(x); x += dpp<ROR4>(x); x += dpp<ROR2>(x); x += dpp<ROR1>(x); return x;
}
__device__ __forceinline__ float swap32_add(float a, float b) {
  auto t = __builtin_amdgcn_permlane32_swap(__float_as_uint(a), __float_as_uint(b), false, false);
  return __uint_as_float(t[0]) + __uint_as_float(t[1]);
}
__device__ __forceinline__ float swap16_add(float a, float b) {
  auto t = __builtin_amdgcn_permlane16_swap(__float_as_uint(a), __float_as_uint(b), false, false);
  return __uint_as_float(t[0]) + __uint_as_float(t[1]);
}
__device__ __forceinline__ float xrow_sum(float x) { x = swap16_add(x, x); return swap32_add(x, x); }
__device__ __forceinline__ float xrow_max(float x) {
  auto s = __builtin_amdgcn_permlane16_swap(__float_as_uint(x), __float_as_uint(x), false, false);
  x = fmaxf(__uint_as_float(s[0]), __uint_as_float(s[1]));
  auto t = __builtin_amdgcn_permlane32_swap(__float_as_uint(x), __float_as_uint(x), false, false);
  return fmaxf(__uint_as_float(t[0]), __uint_as_float(t[1]));
}
__device__ __forceinline__ float wave_sum(float x) { return xrow_sum(row16_sum(x)); }
__device__ __forceinline__ float rbf(float x) { return (float)(__bf16)x; }
__device__ __forceinline__ float bflo(unsigned v) { return __uint_as_float(v << 16); }
__device__ __forceinline__ float bfhi(unsigned v) { return __uint_as_float(v & 0xffff0000u); }
__device__ __forceinline__ unsigned packbf(float lo, float hi) {
  bf16x2 r = {(__bf16)lo, (__bf16)hi};
  return __builtin_bit_cast(unsigned, r);
}
__device__ __forceinline__ u32x4 ldnt(const void* p) { return __builtin_nontemporal_load(reinterpret_cast<const u32x4*>(p)); }
__device__ __forceinline__ u32x4 ld16(const void* p) { return *reinterpret_cast<const u32x4*>(p); }
__device__ __forceinline__ f32x4 mfma(u32x4 a, u32x4 b, f32x4 c) {
  return __builtin_amdgcn_mfma_f32_16x16x32_bf16(__builtin_bit_cast(bf16x8, a), __builtin_bit_cast(bf16x8, b), c, 0, 0, 0);
}
__device__ __forceinline__ u32x4 diag(unsigned x, u32x4 mask) { return u32x4{x & mask[0], x & mask[1], x & mask[2], x & mask[3]}; }

__device__ __forceinline__ void ln_stats(u32x4 xv, float& mb, float& den) {
  float xf[8];
#pragma unroll
  for (int k = 0; k < 4; ++k) { xf[2 * k] = bflo(xv[k]); xf[2 * k + 1] = bfhi(xv[k]); }
  const float s = ((xf[0] + xf[1]) + (xf[2] + xf[3])) + ((xf[4] + xf[5]) + (xf[6] + xf[7]));
  const float mean = wave_sum(s) * (1.f / DM);
  float q0 = 0.f, q1 = 0.f;
#pragma unroll
  for (int k = 0; k < 4; ++k) {
    const float d0 = xf[2 * k] - mean, d1 = xf[2 * k + 1] - mean;
    q0 = fmaf(d0, d0, q0); q1 = fmaf(d1, d1, q1);
  }
  const float var = wave_sum(q0 + q1) * (1.f / DM);
  mb = rbf(mean);
  den = rbf(sqrtf(rbf(rbf(var) + 1e-5f)));
}
__device__ __forceinline__ float gelu_tanh(float x) {
  const float u2 = 2.f * 0.7978845608028654f * (x + 0.044715f * x * x * x);
  const float th = 1.f - 2.f / (1.f + __expf(u2));
  return 0.5f * x * (1.f + th);
}

constexpr int L_WO = 0;
constexpr int L_QP = L_WO + NWAVE * 16384;
constexpr int L_QB = L_QP + NWAVE * 512;
constexpr int L_OW = L_QB + NWAVE * 256;
constexpr int L_ML = L_OW + NWAVE * 512;
constexpr int L_END = L_ML + NWAVE * 8;

__global__ void __launch_bounds__(576)
k_main(const u16* __restrict__ x, const u16* __restrict__ kc, const u16* __restrict__ vc, const u16* __restrict__ wq,
       const u16* __restrict__ wo, const u16* __restrict__ wg, const u16* __restrict__ wu, const u16* __restrict__ wd,
       unsigned char* __restrict__ ws) {
  __shared__ __attribute__((aligned(16))) unsigned char lds[L_END];
  const int tid = threadIdx.x, lane = tid & 63, wave = __builtin_amdgcn_readfirstlane(tid >> 6);
  const int r = lane >> 4, c = lane & 15;
  const int bid = blockIdx.x, g8 = bid & 7, i = bid >> 3;
  const int h = g8 * 4 + (i >> 3), b = (i >> 2) & 1, s = i & 3;
  u32x4 mask;
#pragma unroll
  for (int k = 0; k < 4; ++k) mask[k] = ((c & 7) >> 1) == k ? ((c & 1) ? 0xffff0000u : 0x0000ffffu) : 0u;

  if (wave == NWAVE) {
    const int uq = g8 + 8 * (i >> 2);
    if (uq < NUNIT / 4) {
      const int unit = uq * 4 + (i & 3);
      const u32x4 xv0 = ld16(x + 8 * lane), xv1 = ld16(x + DM + 8 * lane);
      u32x4 gv[8], uv[8], dv[8];
#pragma unroll
      for (int g = 0; g < 8; ++g) gv[g] = ld16(wg + (size_t)(8 * lane + g) * FF + unit * 8);
#pragma unroll
      for (int g = 0; g < 8; ++g) uv[g] = ld16(wu + (size_t)(8 * lane + g) * FF + unit * 8);
#pragma unroll
      for (int g = 0; g < 8; ++g) dv[g] = ld16(wd + (size_t)(unit * 8 + 4 * (g >> 2) + r) * DM + (g & 3) * 128 + 8 * c);
      __builtin_amdgcn_sched_barrier(0);
      __builtin_amdgcn_s_barrier();
      __builtin_amdgcn_sched_barrier(0);
      unsigned xp0[4], xp1[4];
      {
        float mb, den;
        ln_stats(xv0, mb, den);
#pragma unroll
        for (int k = 0; k < 4; ++k) xp0[k] = packbf(rbf(bflo(xv0[k]) - mb) / den, rbf(bfhi(xv0[k]) - mb) / den);
        ln_stats(xv1, mb, den);
#pragma unroll
        for (int k = 0; k < 4; ++k) xp1[k] = packbf(rbf(bflo(xv1[k]) - mb) / den, rbf(bfhi(xv1[k]) - mb) / den);
      }
      float f[2][2][8];
#pragma unroll
      for (int k = 0; k < 32; ++k) (&f[0][0][0])[k] = 0.f;
#pragma unroll
      for (int m = 0; m < 4; ++m)
#pragma unroll
        for (int k = 0; k < 4; ++k) {
          const unsigned glo = __builtin_amdgcn_perm(gv[2 * m + 1][k], gv[2 * m][k], 0x05040100u);
          const unsigned ghi = __builtin_amdgcn_perm(gv[2 * m + 1][k], gv[2 * m][k], 0x07060302u);
          const unsigned ulo = __builtin_amdgcn_perm(uv[2 * m + 1][k], uv[2 * m][k], 0x05040100u);
          const unsigned uhi = __builtin_amdgcn_perm(uv[2 * m + 1][k], uv[2 * m][k], 0x07060302u);
          f[0][0][2 * k] = dot2(glo, xp0[m], f[0][0][2 * k]); f[0][0][2 * k + 1] = dot2(ghi, xp0[m], f[0][0][2 * k + 1]);
          f[0][1][2 * k] = dot2(glo, xp1[m], f[0][1][2 * k]); f[0][1][2 * k + 1] = dot2(ghi, xp1[m], f[0][1][2 * k + 1]);
          f[1][0][2 * k] = dot2(ulo, xp0[m], f[1][0][2 * k]); f[1][0][2 * k + 1] = dot2(uhi, xp0[m], f[1][0][2 * k + 1]);
          f[1][1][2 * k] = dot2(ulo, xp1[m], f[1][1][2 * k]); f[1][1][2 * k + 1] = dot2(uhi, xp1[m], f[1][1][2 * k + 1]);
        }
      float gu[2];
#pragma unroll
      for (int m = 0; m < 2; ++m) {
        float a2[2][2];
#pragma unroll
        for (int bb = 0; bb < 2; ++bb)
#pragma unroll
          for (int j2 = 0; j2 < 2; ++j2) {
            const float e0 = swap32_add(f[m][bb][4 * j2 + 0], f[m][bb][4 * j2 + 2]);
            const float e1 = swap32_add(f[m][bb][4 * j2 + 1], f[m][bb][4 * j2 + 3]);
            a2[bb][j2] = swap16_add(e0, e1);
          }
        float a3[2];
#pragma unroll
        for (int j2 = 0; j2 < 2; ++j2) {
          const float keep = (c & 8) ? a2[1][j2] : a2[0][j2], send = (c & 8) ? a2[0][j2] : a2[1][j2];
          a3[j2] = keep + dpp<ROR8>(send);
        }
        const float keep = (c & 4) ? a3[1] : a3[0], send = (c & 4) ? a3[0] : a3[1];
        float a4 = keep + dpp<HMIR>(send);
        a4 += dpp<XOR1>(a4); a4 += dpp<XOR2>(a4);
        gu[m] = a4;
      }
      const float hv = rbf(gelu_tanh(rbf(gu[0]))) * rbf(gu[1]);
      const unsigned X = packbf(hv, hv), Xo = dppu<HMIR>(X);
      const u32x4 A0 = diag((c & 4) ? Xo : X, mask), A1 = diag((c & 4) ? X : Xo, mask);
      float* fp = reinterpret_cast<float*>(ws + WS_F) + ((size_t)(r >> 1) * NUNIT + unit) * DM + 8 * c + 4 * (r & 1);
#pragma unroll
      for (int sg = 0; sg < 4; ++sg) {
        f32x4 d = mfma(A0, dv[sg], f32x4{0.f, 0.f, 0.f, 0.f});
        d = mfma(A1, dv[4 + sg], d);
        *reinterpret_cast<f32x4*>(fp + sg * 128) = d;
      }
    } else {
      __builtin_amdgcn_s_barrier();
    }
    {
      const u16* src = wo + (size_t)(h * DH + 4 * r) * DM + 8 * c;
#pragma unroll 1
      for (int w = 0; w < NWAVE; ++w) {
#pragma unroll
        for (int k = 0; k < 16; ++k)
          __builtin_amdgcn_global_load_lds(reinterpret_cast<const unsigned*>(src + (size_t)(16 * w + (k >> 2)) * DM + (k & 3) * 128),
                                           reinterpret_cast<unsigned*>(lds + L_WO + w * 16384 + k * 1024), 16, 0, 0);
      }
    }
    asm volatile("s_waitcnt vmcnt(0)" ::: "memory");
    __builtin_amdgcn_s_barrier();
    return;
  }

  const u32x4 xv = ld16(x + b * DM + 8 * lane);
  const u16 xe = x[b * DM + wave * 64 + lane];
  __builtin_amdgcn_sched_barrier(0);
  u32x4 K[8], V[8];
  const size_t kvoff = ((size_t)(b * HQ + h) * SK + (size_t)s * KPB + wave * 32) * DH;
  const unsigned char* kp = reinterpret_cast<const unsigned char*>(kc + kvoff) + c * 256 + r * 16;
  const unsigned char* vp = reinterpret_cast<const unsigned char*>(vc + kvoff) + r * 1024 + c * 16;
  constexpr int GSTR = NWAVE * 32 * DH * 2;
#define KOFF(t) (((t) >> 2) * 4096 + ((t) & 3) * 64)
#define VOFF(t) (((t) >> 2) * 4096 + ((t) & 3) * 256)
  u32x4 qf[4];
  {
    u32x4 wqv[16];
    const u16* p = wq + (size_t)(wave * 64 + 16 * r) * (HQ * DH) + h * DH + 8 * c;
#pragma unroll
    for (int t = 0; t < 16; ++t) wqv[t] = ld16(p + (size_t)t * (HQ * DH));
    __builtin_amdgcn_sched_barrier(0);
#pragma unroll
    for (int t = 0; t < 8; ++t) K[t] = ldnt(kp + KOFF(t));
#pragma unroll
    for (int t = 0; t < 8; ++t) V[t] = ldnt(vp + VOFF(t));
    __builtin_amdgcn_sched_barrier(0);
    float mb, den;
    ln_stats(xv, mb, den);
    const float xn = rbf(__uint_as_float((unsigned)xe << 16) - mb) / den;
    const unsigned X = packbf(xn, xn);
    f32x4 qa = {0.f, 0.f, 0.f, 0.f};
#define QSTEP(t) qa = mfma(diag(dppu<0x150 + t>(X), mask), wqv[t], qa);
    QSTEP(0) QSTEP(1) QSTEP(2) QSTEP(3) QSTEP(4) QSTEP(5) QSTEP(6) QSTEP(7)
    QSTEP(8) QSTEP(9) QSTEP(10) QSTEP(11) QSTEP(12) QSTEP(13) QSTEP(14) QSTEP(15)
#undef QSTEP
    if (r < 2) *reinterpret_cast<f32x4*>(lds + L_QP + wave * 512 + (8 * c + 4 * r) * 4) = qa;
    __syncthreads();
    float q0 = 0.f, q1 = 0.f;
#pragma unroll
    for (int w = 0; w < NWAVE; ++w) {
      const float2 v = *reinterpret_cast<const float2*>(lds + L_QP + w * 512 + lane * 8);
      q0 += v.x; q1 += v.y;
    }
    *reinterpret_cast<unsigned*>(lds + L_QB + wave * 256 + lane * 4) = packbf(q0, q1);
    __builtin_amdgcn_fence(__ATOMIC_RELEASE, "wavefront");
    __builtin_amdgcn_wave_barrier();
    __builtin_amdgcn_fence(__ATOMIC_ACQUIRE, "wavefront");
#pragma unroll
    for (int k = 0; k < 4; ++k) qf[k] = *reinterpret_cast<const u32x4*>(lds + L_QB + wave * 256 + k * 64 + r * 16);
  }

  float m = -INFINITY, l = 0.f;
  f32x4 acc = {0.f, 0.f, 0.f, 0.f};
#define PROCESS(REFILL)                                                                  \
  {                                                                                      \
    f32x4 S0 = {0.f, 0.f, 0.f, 0.f}, S1 = {0.f, 0.f, 0.f, 0.f};                          \
    _Pragma("unroll") for (int t = 0; t < 4; ++t) {                                      \
      S0 = mfma(K[t], qf[t], S0);                                                        \
      if (REFILL) K[t] = ldnt(kp + KOFF(t));                                             \
      __builtin_amdgcn_sched_barrier(0);                                                 \
    }                                                                                    \
    _Pragma("unroll") for (int t = 0; t < 4; ++t) {                                      \
      S1 = mfma(K[4 + t], qf[t], S1);                                                    \
      if (REFILL) K[4 + t] = ldnt(kp + KOFF(4 + t));                                     \
      __builtin_amdgcn_sched_barrier(0);                                                 \
    }                                                                                    \
    float sb[4];                                                                         \
    _Pragma("unroll") for (int k = 0; k < 4; ++k) sb[k] = rbf(rbf((c & 1) ? S1[k] : S0[k]) * SCALE); \
    float mx = fmaxf(fmaxf(sb[0], sb[1]), fmaxf(sb[2], sb[3]));                          \
    mx = fmaxf(mx, dpp<XOR1>(mx));                                                       \
    mx = fmaxf(xrow_max(mx), m);                                                         \
    const float alpha = __builtin_amdgcn_exp2f((m - mx) * LOG2E);                        \
    const float nb = -mx * LOG2E;                                                        \
    m = mx; l *= alpha; acc *= alpha;                                                    \
    unsigned X[4];                                                                       \
    _Pragma("unroll") for (int k = 0; k < 4; ++k) {                                      \
      const float p = __builtin_amdgcn_exp2f(fmaf(sb[k], LOG2E, nb));                    \
      l += p;                                                                            \
      const float y = (c & 8) ? p - rbf(p) : p;                                          \
      X[k] = packbf(y, y);                                                               \
    }                                                                                    \
    __builtin_amdgcn_sched_barrier(0);                                                   \
    _Pragma("unroll") for (int t = 0; t < 4; ++t) {                                      \
      acc = mfma(diag(dppu<0x00>(X[t]), mask), V[t], acc);                               \
      if (REFILL) V[t] = ldnt(vp + VOFF(t));                                             \
      __builtin_amdgcn_sched_barrier(0);                                                 \
    }                                                                                    \
    _Pragma("unroll") for (int t = 0; t < 4; ++t) {                                      \
      acc = mfma(diag(dppu<0x55>(X[t]), mask), V[4 + t], acc);                           \
      if (REFILL) V[4 + t] = ldnt(vp + VOFF(4 + t));                                     \
      __builtin_amdgcn_sched_barrier(0);                                                 \
    }                                                                                    \
  }
#pragma unroll 1
  for (int g = 0; g < NGROUP - 1; ++g) {
    kp += GSTR; vp += GSTR;
    PROCESS(true)
  }
  PROCESS(false)
#undef PROCESS

  {
    l += dpp<XOR1>(l);
    l = xrow_sum(l);
    f32x4 o;
#pragma unroll
    for (int k = 0; k < 4; ++k) o[k] = swap32_add(acc[k], acc[k]);
    if (r < 2) *reinterpret_cast<f32x4*>(lds + L_OW + wave * 512 + (8 * c + 4 * r) * 4) = o;
    if (lane == 0) *reinterpret_cast<float2*>(lds + L_ML + wave * 8) = make_float2(m, l);
  }
  __syncthreads();
  float Mb = -INFINITY, Lb = 0.f;
  {
    u32x4 wf[16];
#pragma unroll
    for (int k = 0; k < 16; ++k) wf[k] = *reinterpret_cast<const u32x4*>(lds + L_WO + wave * 16384 + k * 1024 + lane * 16);
    float2 mlw[NWAVE];
#pragma unroll
    for (int w = 0; w < NWAVE; ++w) { mlw[w] = *reinterpret_cast<const float2*>(lds + L_ML + w * 8); Mb = fmaxf(Mb, mlw[w].x); }
    float o = 0.f;
#pragma unroll
    for (int w = 0; w < NWAVE; ++w) {
      const float cw = __builtin_amdgcn_exp2f((mlw[w].x - Mb) * LOG2E);
      Lb = fmaf(cw, mlw[w].y, Lb);
      o = fmaf(cw, *reinterpret_cast<const float*>(lds + L_OW + w * 512 + (wave * 16 + 4 * r + (c & 3)) * 4), o);
    }
    const float y = (c & 8) ? o - rbf(o) : o;
    const unsigned X = packbf(y, y);
    const u32x4 A0 = diag(dppu<0x00>(X), mask), A1 = diag(dppu<0x55>(X), mask);
    const u32x4 A2 = diag(dppu<0xAA>(X), mask), A3 = diag(dppu<0xFF>(X), mask);
#pragma unroll
    for (int sg = 0; sg < 4; ++sg) {
      f32x4 d = mfma(A0, wf[sg], f32x4{0.f, 0.f, 0.f, 0.f});
      d = mfma(A1, wf[4 + sg], d);
      d = mfma(A2, wf[8 + sg], d);
      d = mfma(A3, wf[12 + sg], d);
      *reinterpret_cast<f32x4*>(lds + L_WO + wave * 16384 + (r >> 1) * 2048 + (sg * 128 + 8 * c + 4 * (r & 1)) * 4) = d;
    }
  }
  __syncthreads();
  {
    float y = 0.f;
#pragma unroll
    for (int w = 0; w < NWAVE; ++w)
      y += *reinterpret_cast<const float*>(lds + L_WO + w * 16384 + tid * 4) + *reinterpret_cast<const float*>(lds + L_WO + w * 16384 + 2048 + tid * 4);
    const int slot = b * NSLOT + h * NSPLIT + s;
    reinterpret_cast<float*>(ws + WS_Y)[(size_t)slot * DM + tid] = y;
    if (tid == 0) *reinterpret_cast<float2*>(ws + WS_ML + (size_t)slot * 8) = make_float2(Mb * LOG2E, Lb);
  }
}

__global__ void __launch_bounds__(512)
k_final(const u16* __restrict__ x, const unsigned char* __restrict__ ws, u16* __restrict__ out) {
  __shared__ __attribute__((aligned(16))) float red[NWAVE][2][8];
  const int tid = threadIdx.x, lane = tid & 63, wave = tid >> 6;
  const int b = blockIdx.x & 1, cg = blockIdx.x >> 1;
  const int row = tid >> 1, half = tid & 1;
  f32x4 sa = {0.f, 0.f, 0.f, 0.f}, sf = {0.f, 0.f, 0.f, 0.f};
  u16 xo = 0;
  if (tid < 8) xo = x[b * DM + cg * 8 + tid];
  if (row < NUNIT) sf = *reinterpret_cast<const f32x4*>(ws + WS_F + (((size_t)b * NUNIT + row) * DM + cg * 8 + half * 4) * 4);
  if (row < NSLOT) {
    const f32x4 y = *reinterpret_cast<const f32x4*>(ws + WS_Y + (((size_t)b * NSLOT + row) * DM + cg * 8 + half * 4) * 4);
    const f32x4* mlp = reinterpret_cast<const f32x4*>(ws + WS_ML + ((size_t)b * NSLOT + (row & ~3)) * 8);
    const f32x4 a = mlp[0], c4 = mlp[1];
    const float M = fmaxf(fmaxf(a[0], a[2]), fmaxf(c4[0], c4[2]));
    const float e0 = __builtin_amdgcn_exp2f(a[0] - M), e1 = __builtin_amdgcn_exp2f(a[2] - M);
    const float e2 = __builtin_amdgcn_exp2f(c4[0] - M), e3 = __builtin_amdgcn_exp2f(c4[2] - M);
    const float L = fmaf(e0, a[1], fmaf(e1, a[3], fmaf(e2, c4[1], e3 * c4[3])));
    const int sp = row & 3;
    const float e = sp == 0 ? e0 : sp == 1 ? e1 : sp == 2 ? e2 : e3;
    sa = y * (e / L);
  }
  float v[8] = {sa[0], sa[1], sa[2], sa[3], sf[0], sf[1], sf[2], sf[3]};
#pragma unroll
  for (int k = 0; k < 8; ++k) {
    float t = v[k];
    t += dpp<XOR2>(t); t += dpp<ROR4>(t); t += dpp<ROR8>(t);
    v[k] = xrow_sum(t);
  }
  if (lane < 2) {
    *reinterpret_cast<f32x4*>(&red[wave][lane][0]) = f32x4{v[0], v[1], v[2], v[3]};
    *reinterpret_cast<f32x4*>(&red[wave][lane][4]) = f32x4{v[4], v[5], v[6], v[7]};
  }
  __syncthreads();
  if (tid < 8) {
    float s = 0.f, f = 0.f;
#pragma unroll
    for (int w = 0; w < NWAVE; ++w) { s += red[w][tid >> 2][tid & 3]; f += red[w][tid >> 2][4 + (tid & 3)]; }
    s = rbf(s); f = rbf(f);
    const float g = rbf(f / (1.f + __expf(-f)));
    const float o = __uint_as_float((unsigned)xo << 16) + rbf(s * g);
    out[b * DM + cg * 8 + tid] = __builtin_bit_cast(u16, (__bf16)o);
  }
}

extern "C" void kernel_launch(void* const* d_in, const int* in_sizes, int n_in, void* d_out, int out_size, void* d_ws,
                              size_t ws_size, hipStream_t stream) {
  const u16* x = (const u16*)d_in[0];
  const u16* kc = (const u16*)d_in[1];
  const u16* vc = (const u16*)d_in[2];
  const u16* wq = (const u16*)d_in[3];
  const u16* wo = (const u16*)d_in[4];
  const u16* wg = (const u16*)d_in[6];
  const u16* wu = (const u16*)d_in[7];
  const u16* wd = (const u16*)d_in[8];
  k_main<<<256, 576, 0, stream>>>(x, kc, vc, wq, wo, wg, wu, wd, (unsigned char*)d_ws);
  k_final<<<128, 512, 0, stream>>>(x, (const unsigned char*)d_ws, (u16*)d_out);
}
```

```cpp
#include <hip/hip_runtime.h>
#include <hip/hip_bf16.h>
#include <stdint.h>

using u32x4 = __attribute__((ext_vector_type(4))) unsigned;
using f32x4 = __attribute__((ext_vector_type(4))) float;
using bf16x2 = __attribute__((ext_vector_type(2))) __bf16;
using bf16x8 = __attribute__((ext_vector_type(8))) __bf16;
typedef unsigned short u16;

constexpr int DM = 512, HQ = 32, SK = 8192, DH = 128, FF = 1376;
constexpr int NSPLIT = 4;
constexpr int NWAVE = 8;
constexpr int NTILE = SK / 16;
#define TOTH 133
constexpr int T_OTHER = TOTH;
constexpr int NUNIT = FF / 32;
constexpr int NSLOT = HQ * NSPLIT;
constexpr float LOG2E = 1.4426950408889634f;
constexpr float SCALE = 0.08838834764831845f;

constexpr size_t WS_Y = 0;
constexpr size_t WS_ML = WS_Y + (size_t)2 * NSLOT * DM * 4;
constexpr size_t WS_F = WS_ML + (size_t)2 * NSLOT * 2 * 4;

__device__ __forceinline__ float dot2(unsigned a, unsigned b, float acc) {
  return __builtin_amdgcn_fdot2_f32_bf16(__builtin_bit_cast(bf16x2, a), __builtin_bit_cast(bf16x2, b), acc, false);
}
template <int CTRL> __device__ __forceinline__ float dpp(float x) {
  return __builtin_bit_cast(float, __builtin_amdgcn_mov_dpp(__builtin_bit_cast(int, x), CTRL, 0xf, 0xf, true));
}
template <int CTRL> __device__ __forceinline__ unsigned dppu(unsigned x) {
  return (unsigned)__builtin_amdgcn_mov_dpp((int)x, CTRL, 0xf, 0xf, true);
}
constexpr int ROR8 = 0x128, ROR4 = 0x124, ROR2 = 0x122, ROR1 = 0x121, XOR1 = 0xB1, XOR2 = 0x4E, HMIR = 0x141;
__device__ __forceinline__ float row16_sum(float x) {
  x += dpp<ROR8>(x); x += dpp<ROR4>(x); x += dpp<ROR2>(x); x += dpp<ROR1>(x); return x;
}
__device__ __forceinline__ float swap32_add(float a, float b) {
  auto t = __builtin_amdgcn_permlane32_swap(__float_as_uint(a), __float_as_uint(b), false, false);
  return __uint_as_float(t[0]) + __uint_as_float(t[1]);
}
__device__ __forceinline__ float swap16_add(float a, float b) {
  auto t = __builtin_amdgcn_permlane16_swap(__float_as_uint(a), __float_as_uint(b), false, false);
  return __uint_as_float(t[0]) + __uint_as_float(t[1]);
}
__device__ __forceinline__ float xrow_sum(float x) { x = swap16_add(x, x); return swap32_add(x, x); }
__device__ __forceinline__ float xrow_max(float x) {
  auto s = __builtin_amdgcn_permlane16_swap(__float_as_uint(x), __float_as_uint(x), false, false);
  x = fmaxf(__uint_as_float(s[0]), __uint_as_float(s[1]));
  auto t = __builtin_amdgcn_permlane32_swap(__float_as_uint(x), __float_as_uint(x), false, false);
  return fmaxf(__uint_as_float(t[0]), __uint_as_float(t[1]));
}
__device__ __forceinline__ float wave_sum(float x) { return xrow_sum(row16_sum(x)); }
__device__ __forceinline__ float rbf(float x) { return (float)(__bf16)x; }
__device__ __forceinline__ float bflo(unsigned v) { return __uint_as_float(v << 16); }
__device__ __forceinline__ float bfhi(unsigned v) { return __uint_as_float(v & 0xffff0000u); }
__device__ __forceinline__ unsigned packbf(float lo, float hi) {
  bf16x2 r = {(__bf16)lo, (__bf16)hi};
  return __builtin_bit_cast(unsigned, r);
}
__device__ __forceinline__ u32x4 ldnt(const void* p) { return __builtin_nontemporal_load(reinterpret_cast<const u32x4*>(p)); }
__device__ __forceinline__ u32x4 ld16(const void* p) { return *reinterpret_cast<const u32x4*>(p); }
__device__ __forceinline__ f32x4 mfma(u32x4 a, u32x4 b, f32x4 c) {
  return __builtin_amdgcn_mfma_f32_16x16x32_bf16(__builtin_bit_cast(bf16x8, a), __builtin_bit_cast(bf16x8, b), c, 0, 0, 0);
}
__device__ __forceinline__ u32x4 diag(unsigned x, u32x4 mask) { return u32x4{x & mask[0], x & mask[1], x & mask[2], x & mask[3]}; }

__device__ __forceinline__ void ln_stats(u32x4 xv, float& mb, float& den) {
  float xf[8];
#pragma unroll
  for (int k = 0; k < 4; ++k) { xf[2 * k] = bflo(xv[k]); xf[2 * k + 1] = bfhi(xv[k]); }
  const float s = ((xf[0] + xf[1]) + (xf[2] + xf[3])) + ((xf[4] + xf[5]) + (xf[6] + xf[7]));
  const float mean = wave_sum(s) * (1.f / DM);
  float q0 = 0.f, q1 = 0.f;
#pragma unroll
  for (int k = 0; k < 4; ++k) {
    const float d0 = xf[2 * k] - mean, d1 = xf[2 * k + 1] - mean;
    q0 = fmaf(d0, d0, q0); q1 = fmaf(d1, d1, q1);
  }
  const float var = wave_sum(q0 + q1) * (1.f / DM);
  mb = rbf(mean);
  den = rbf(sqrtf(rbf(rbf(var) + 1e-5f)));
}
__device__ __forceinline__ float gelu_tanh(float x) {
  const float u2 = 2.f * 0.7978845608028654f * (x + 0.044715f * x * x * x);
  const float th = 1.f - 2.f / (1.f + __expf(u2));
  return 0.5f * x * (1.f + th);
}

constexpr int L_WO = 0;
constexpr int L_QP = L_WO + NWAVE * 16384;
constexpr int L_QB = L_QP + NWAVE * 512;
constexpr int L_OW = L_QB + NWAVE * 256;
constexpr int L_ML = L_OW + NWAVE * 512;
constexpr int L_FP = L_ML + NWAVE * 8;
constexpr int L_HX = L_FP + NWAVE * 512;
constexpr int L_END = L_HX + 4 * 256;

__global__ void __launch_bounds__(512)
k_main(const u16* __restrict__ x, const u16* __restrict__ kc, const u16* __restrict__ vc, const u16* __restrict__ wq,
       const u16* __restrict__ wo, const u16* __restrict__ wg, const u16* __restrict__ wu, const u16* __restrict__ wd,
       unsigned char* __restrict__ ws) {
  __shared__ __attribute__((aligned(16))) unsigned char lds[L_END];
  const int tid = threadIdx.x, lane = tid & 63, wave = __builtin_amdgcn_readfirstlane(tid >> 6);
  const int r = lane >> 4, c = lane & 15;
  const int bid = blockIdx.x, g8 = bid & 7, i = bid >> 3;
  const int h = g8 * 4 + (i >> 3), b = (i >> 2) & 1, s = i & 3;
  const int unit = g8 + 8 * (i >> 2);
  const bool pair_ffn = unit < NUNIT, has_ffn = pair_ffn && s == 3;
  const int t0 = s * (pair_ffn ? T_OTHER : NTILE / 4);
  const int nt = (s == 3 ? NTILE : t0 + (pair_ffn ? T_OTHER : NTILE / 4)) - t0;
  const int n = (nt - wave + 7) >> 3;

  {
    const u16* src = wo + (size_t)(h * DH + 16 * wave + 4 * r) * DM + 8 * c;
    const unsigned dst = (unsigned)(uintptr_t)(lds + L_WO) + wave * 16384;
#pragma unroll
    for (int k = 0; k < 16; ++k) {
      unsigned keep;
      asm volatile("s_mov_b32 %0, m0\n\ts_mov_b32 m0, %2\n\ts_nop 0\n\tglobal_load_lds_dwordx4 %1, off nt\n\ts_mov_b32 m0, %0"
                   : "=&s"(keep) : "v"(src + (size_t)(k >> 2) * DM + (k & 3) * 128), "s"(dst + k * 1024) : "memory");
    }
  }
  u32x4 mask;
#pragma unroll
  for (int k = 0; k < 4; ++k) mask[k] = ((c & 7) >> 1) == k ? ((c & 1) ? 0xffff0000u : 0x0000ffffu) : 0u;

  const u32x4 xv = ld16(x + b * DM + 8 * lane);
  const u16 xe = x[b * DM + wave * 64 + lane];
  __builtin_amdgcn_sched_barrier(0);
  u32x4 Ka[4], Va[4], Kb[4], Vb[4];
  const size_t kvoff = ((size_t)(b * HQ + h) * SK + (size_t)(t0 + wave) * 16) * DH;
  const unsigned char* kp = reinterpret_cast<const unsigned char*>(kc + kvoff) + c * 256 + r * 16;
  const unsigned char* vp = reinterpret_cast<const unsigned char*>(vc + kvoff) + r * 1024 + c * 16;
  constexpr int TSTR = NWAVE * 4096;
#define LDK(KR, off) _Pragma("unroll") for (int t = 0; t < 4; ++t) KR[t] = ldnt(kp + (off) + t * 64);
#define LDV(VR, off) _Pragma("unroll") for (int t = 0; t < 4; ++t) VR[t] = ldnt(vp + (off) + t * 256);
#define SB __builtin_amdgcn_sched_barrier(0);
  u32x4 qf[4];
  {
    u32x4 xo, gv[4], uv[4], dv[8];
    uint2 xr0, xr1;
    if (has_ffn) {
      xo = ld16(x + (1 - b) * DM + 8 * lane);
      xr0 = *reinterpret_cast<const uint2*>(x + wave * 64 + 4 * (lane >> 2));
      xr1 = *reinterpret_cast<const uint2*>(x + DM + wave * 64 + 4 * (lane >> 2));
      const size_t fo = (size_t)(wave * 64 + 4 * (lane >> 2)) * FF + unit * 32 + 8 * (lane & 3);
#pragma unroll
      for (int t = 0; t < 4; ++t) gv[t] = ldnt(wg + fo + (size_t)t * FF);
#pragma unroll
      for (int t = 0; t < 4; ++t) uv[t] = ldnt(wu + fo + (size_t)t * FF);
    }
    SB
    u32x4 wqv[16];
    const u16* p = wq + (size_t)(wave * 64 + 16 * r) * (HQ * DH) + h * DH + 8 * c;
#pragma unroll
    for (int t = 0; t < 16; ++t) wqv[t] = ldnt(p + (size_t)t * (HQ * DH));
    SB
    if (has_ffn && wave < 4) {
#pragma unroll
      for (int t = 0; t < 8; ++t) dv[t] = ldnt(wd + (size_t)(unit * 32 + 4 * t + r) * DM + wave * 128 + 8 * c);
    }
    SB
    LDK(Ka, 0) SB LDV(Va, 0) SB LDK(Kb, TSTR) SB LDV(Vb, TSTR) SB
    float mb, den;
    ln_stats(xv, mb, den);
    if (has_ffn) {
      float mo, deo;
      ln_stats(xo, mo, deo);
      const float m0 = b ? mo : mb, d0 = b ? deo : den, m1 = b ? mb : mo, d1 = b ? den : deo;
      unsigned xp0[2], xp1[2];
      xp0[0] = packbf(rbf(bflo(xr0.x) - m0) / d0, rbf(bfhi(xr0.x) - m0) / d0);
      xp0[1] = packbf(rbf(bflo(xr0.y) - m0) / d0, rbf(bfhi(xr0.y) - m0) / d0);
      xp1[0] = packbf(rbf(bflo(xr1.x) - m1) / d1, rbf(bfhi(xr1.x) - m1) / d1);
      xp1[1] = packbf(rbf(bflo(xr1.y) - m1) / d1, rbf(bfhi(xr1.y) - m1) / d1);
      float f[2][2][8];
#pragma unroll
      for (int k = 0; k < 32; ++k) (&f[0][0][0])[k] = 0.f;
#pragma unroll
      for (int m = 0; m < 2; ++m)
#pragma unroll
        for (int k = 0; k < 4; ++k) {
          const unsigned glo = __builtin_amdgcn_perm(gv[2 * m + 1][k], gv[2 * m][k], 0x05040100u);
          const unsigned ghi = __builtin_amdgcn_perm(gv[2 * m + 1][k], gv[2 * m][k], 0x07060302u);
          const unsigned ulo = __builtin_amdgcn_perm(uv[2 * m + 1][k], uv[2 * m][k], 0x05040100u);
          const unsigned uhi = __builtin_amdgcn_perm(uv[2 * m + 1][k], uv[2 * m][k], 0x07060302u);
          f[0][0][2 * k] = dot2(glo, xp0[m], f[0][0][2 * k]); f[0][0][2 * k + 1] = dot2(ghi, xp0[m], f[0][0][2 * k + 1]);
          f[0][1][2 * k] = dot2(glo, xp1[m], f[0][1][2 * k]); f[0][1][2 * k + 1] = dot2(ghi, xp1[m], f[0][1][2 * k + 1]);
          f[1][0][2 * k] = dot2(ulo, xp0[m], f[1][0][2 * k]); f[1][0][2 * k + 1] = dot2(uhi, xp0[m], f[1][0][2 * k + 1]);
          f[1][1][2 * k] = dot2(ulo, xp1[m], f[1][1][2 * k]); f[1][1][2 * k + 1] = dot2(uhi, xp1[m], f[1][1][2 * k + 1]);
        }
      float gu[2];
#pragma unroll
      for (int m = 0; m < 2; ++m) {
        float a2[2][2];
#pragma unroll
        for (int bb = 0; bb < 2; ++bb)
#pragma unroll
          for (int j2 = 0; j2 < 2; ++j2) {
            const float e0 = swap32_add(f[m][bb][4 * j2 + 0], f[m][bb][4 * j2 + 2]);
            const float e1 = swap32_add(f[m][bb][4 * j2 + 1], f[m][bb][4 * j2 + 3]);
            a2[bb][j2] = swap16_add(e0, e1);
          }
        float a3[2];
#pragma unroll
        for (int j2 = 0; j2 < 2; ++j2) {
          const float keep = (c & 8) ? a2[1][j2] : a2[0][j2], send = (c & 8) ? a2[0][j2] : a2[1][j2];
          a3[j2] = keep + dpp<ROR8>(send);
        }
        const float s0 = dpp<0x124>(a3[1]), s1 = dpp<0x12C>(a3[0]);
        gu[m] = (c & 4) ? a3[1] + s0 : a3[0] + s1;
      }
      *reinterpret_cast<float2*>(lds + L_FP + wave * 512 + lane * 8) = make_float2(gu[0], gu[1]);
    }
    const float xn = rbf(__uint_as_float((unsigned)xe << 16) - mb) / den;
    const unsigned X = packbf(xn, xn);
    f32x4 qa = {0.f, 0.f, 0.f, 0.f};
#define QSTEP(t) qa = mfma(diag(dppu<0x150 + t>(X), mask), wqv[t], qa);
    QSTEP(0) QSTEP(1) QSTEP(2) QSTEP(3) QSTEP(4) QSTEP(5) QSTEP(6) QSTEP(7)
    QSTEP(8) QSTEP(9) QSTEP(10) QSTEP(11) QSTEP(12) QSTEP(13) QSTEP(14) QSTEP(15)
#undef QSTEP
    if (r < 2) *reinterpret_cast<f32x4*>(lds + L_QP + wave * 512 + (8 * c + 4 * r) * 4) = qa;
    __syncthreads();
    float q0 = 0.f, q1 = 0.f;
#pragma unroll
    for (int w = 0; w < NWAVE; ++w) {
      const float2 v = *reinterpret_cast<const float2*>(lds + L_QP + w * 512 + lane * 8);
      q0 += v.x; q1 += v.y;
    }
    *reinterpret_cast<unsigned*>(lds + L_QB + wave * 256 + lane * 4) = packbf(q0, q1);
    if (has_ffn && wave < 4) {
      float g = 0.f, u = 0.f;
#pragma unroll
      for (int w = 0; w < NWAVE; ++w) {
        const float2 v = *reinterpret_cast<const float2*>(lds + L_FP + w * 512 + lane * 8);
        g += v.x; u += v.y;
      }
      const float hv = rbf(gelu_tanh(rbf(g))) * rbf(u);
      *reinterpret_cast<unsigned*>(lds + L_HX + wave * 256 + (((c >> 3) * 4 + r) * 8 + (c & 3) * 2 + ((c >> 2) & 1)) * 4) = packbf(hv, hv);
    }
    __builtin_amdgcn_fence(__ATOMIC_RELEASE, "wavefront");
    __builtin_amdgcn_wave_barrier();
    __builtin_amdgcn_fence(__ATOMIC_ACQUIRE, "wavefront");
#pragma unroll
    for (int k = 0; k < 4; ++k) qf[k] = *reinterpret_cast<const u32x4*>(lds + L_QB + wave * 256 + k * 64 + r * 16);
    if (has_ffn && wave < 4) {
      const u32x4 h0 = *reinterpret_cast<const u32x4*>(lds + L_HX + wave * 256 + ((c >> 3) * 4 + r) * 32);
      const u32x4 h1 = *reinterpret_cast<const u32x4*>(lds + L_HX + wave * 256 + ((c >> 3) * 4 + r) * 32 + 16);
      f32x4 d = {0.f, 0.f, 0.f, 0.f};
#pragma unroll
      for (int t = 0; t < 4; ++t) d = mfma(diag(h0[t], mask), dv[t], d);
#pragma unroll
      for (int t = 0; t < 4; ++t) d = mfma(diag(h1[t], mask), dv[4 + t], d);
      *reinterpret_cast<f32x4*>(reinterpret_cast<float*>(ws + WS_F) + ((size_t)(r >> 1) * NUNIT + unit) * DM + wave * 128 + 8 * c + 4 * (r & 1)) = d;
    }
  }

  float m = -INFINITY, l = 0.f;
  f32x4 acc = {0.f, 0.f, 0.f, 0.f};
#define STEP(KR, VR, REFILL, off)                                                        \
  {                                                                                      \
    f32x4 S = {0.f, 0.f, 0.f, 0.f};                                                      \
    _Pragma("unroll") for (int t = 0; t < 4; ++t) S = mfma(KR[t], qf[t], S);             \
    SB                                                                                   \
    if (REFILL) { LDK(KR, off) }                                                         \
    SB                                                                                   \
    float sb[4];                                                                         \
    _Pragma("unroll") for (int k = 0; k < 4; ++k) sb[k] = rbf(rbf(S[k]) * SCALE);        \
    float mx = fmaxf(fmaxf(sb[0], sb[1]), fmaxf(sb[2], sb[3]));                          \
    mx = fmaxf(xrow_max(mx), m);                                                         \
    const float alpha = __builtin_amdgcn_exp2f((m - mx) * LOG2E);                        \
    const float nb = -mx * LOG2E;                                                        \
    m = mx; l *= alpha; acc *= alpha;                                                    \
    _Pragma("unroll") for (int k = 0; k < 4; ++k) {                                      \
      const float p = __builtin_amdgcn_exp2f(fmaf(sb[k], LOG2E, nb));                    \
      l += p;                                                                            \
      const float y = (c & 8) ? p - rbf(p) : p;                                          \
      acc = mfma(diag(packbf(y, y), mask), VR[k], acc);                                  \
    }                                                                                    \
    SB                                                                                   \
    if (REFILL) { LDV(VR, off) }                                                         \
    SB                                                                                   \
  }
  int k = 2;
#pragma unroll 1
  for (; k + 1 < n; k += 2) {
    kp += 2 * TSTR; vp += 2 * TSTR;
    STEP(Ka, Va, true, 0)
    STEP(Kb, Vb, true, TSTR)
  }
  if (k < n) {
    STEP(Ka, Va, true, 2 * TSTR)
    STEP(Kb, Vb, false, 0)
    STEP(Ka, Va, false, 0)
  } else {
    STEP(Ka, Va, false, 0)
    STEP(Kb, Vb, false, 0)
  }
#undef STEP

  {
    l = xrow_sum(l);
    f32x4 o;
#pragma unroll
    for (int k2 = 0; k2 < 4; ++k2) o[k2] = swap32_add(acc[k2], acc[k2]);
    if (r < 2) *reinterpret_cast<f32x4*>(lds + L_OW + wave * 512 + (8 * c + 4 * r) * 4) = o;
    if (lane == 0) *reinterpret_cast<float2*>(lds + L_ML + wave * 8) = make_float2(m, l);
  }
  u32x4 wf[16];
#pragma unroll
  for (int k2 = 0; k2 < 16; ++k2) wf[k2] = *reinterpret_cast<const u32x4*>(lds + L_WO + wave * 16384 + k2 * 1024 + lane * 16);
  __syncthreads();
  float Mb = -INFINITY, Lb = 0.f;
  {
    float2 mlw[NWAVE];
#pragma unroll
    for (int w = 0; w < NWAVE; ++w) { mlw[w] = *reinterpret_cast<const float2*>(lds + L_ML + w * 8); Mb = fmaxf(Mb, mlw[w].x); }
    float o = 0.f;
#pragma unroll
    for (int w = 0; w < NWAVE; ++w) {
      const float cw = __builtin_amdgcn_exp2f((mlw[w].x - Mb) * LOG2E);
      Lb = fmaf(cw, mlw[w].y, Lb);
      o = fmaf(cw, *reinterpret_cast<const float*>(lds + L_OW + w * 512 + (wave * 16 + 4 * r + (c & 3)) * 4), o);
    }
    const float y = (c & 8) ? o - rbf(o) : o;
    const unsigned X = packbf(y, y);
    const u32x4 A0 = diag(dppu<0x00>(X), mask), A1 = diag(dppu<0x55>(X), mask);
    const u32x4 A2 = diag(dppu<0xAA>(X), mask), A3 = diag(dppu<0xFF>(X), mask);
#pragma unroll
    for (int sg = 0; sg < 4; ++sg) {
      f32x4 d = mfma(A0, wf[sg], f32x4{0.f, 0.f, 0.f, 0.f});
      d = mfma(A1, wf[4 + sg], d);
      d = mfma(A2, wf[8 + sg], d);
      d = mfma(A3, wf[12 + sg], d);
      *reinterpret_cast<f32x4*>(lds + L_WO + wave * 16384 + (r >> 1) * 2048 + (sg * 128 + 8 * c + 4 * (r & 1)) * 4) = d;
    }
  }
  __syncthreads();
  {
    float y = 0.f;
#pragma unroll
    for (int w = 0; w < NWAVE; ++w)
      y += *reinterpret_cast<const float*>(lds + L_WO + w * 16384 + tid * 4) + *reinterpret_cast<const float*>(lds + L_WO + w * 16384 + 2048 + tid * 4);
    const int slot = b * NSLOT + h * NSPLIT + s;
    reinterpret_cast<float*>(ws + WS_Y)[(size_t)slot * DM + tid] = y;
    if (tid == 0) *reinterpret_cast<float2*>(ws + WS_ML + (size_t)slot * 8) = make_float2(Mb * LOG2E, Lb);
  }
}

__global__ void __launch_bounds__(512)
k_final(const u16* __restrict__ x, const unsigned char* __restrict__ ws, u16* __restrict__ out) {
  __shared__ __attribute__((aligned(16))) float red[NWAVE][2][8];
  const int tid = threadIdx.x, lane = tid & 63, wave = tid >> 6;
  const int b = blockIdx.x & 1, cg = blockIdx.x >> 1;
  const int row = tid >> 1, half = tid & 1;
  f32x4 sa = {0.f, 0.f, 0.f, 0.f}, sf = {0.f, 0.f, 0.f, 0.f};
  u16 xo = 0;
  if (tid < 8) xo = x[b * DM + cg * 8 + tid];
  if (row < NUNIT) sf = *reinterpret_cast<const f32x4*>(ws + WS_F + (((size_t)b * NUNIT + row) * DM + cg * 8 + half * 4) * 4);
  if (row < NSLOT) {
    const f32x4 y = *reinterpret_cast<const f32x4*>(ws + WS_Y + (((size_t)b * NSLOT + row) * DM + cg * 8 + half * 4) * 4);
    const f32x4* mlp = reinterpret_cast<const f32x4*>(ws + WS_ML + ((size_t)b * NSLOT + (row & ~3)) * 8);
    const f32x4 a = mlp[0], c4 = mlp[1];
    const float M = fmaxf(fmaxf(a[0], a[2]), fmaxf(c4[0], c4[2]));
    const float e0 = __builtin_amdgcn_exp2f(a[0] - M), e1 = __builtin_amdgcn_exp2f(a[2] - M);
    const float e2 = __builtin_amdgcn_exp2f(c4[0] - M), e3 = __builtin_amdgcn_exp2f(c4[2] - M);
    const float L = fmaf(e0, a[1], fmaf(e1, a[3], fmaf(e2, c4[1], e3 * c4[3])));
    const int sp = row & 3;
    const float e = sp == 0 ? e0 : sp == 1 ? e1 : sp == 2 ? e2 : e3;
    sa = y * (e / L);
  }
  float v[8] = {sa[0], sa[1], sa[2], sa[3], sf[0], sf[1], sf[2], sf[3]};
#pragma unroll
  for (int k = 0; k < 8; ++k) {
    float t = v[k];
    t += dpp<XOR2>(t); t += dpp<ROR4>(t); t += dpp<ROR8>(t);
    v[k] = xrow_sum(t);
  }
  if (lane < 2) {
    *reinterpret_cast<f32x4*>(&red[wave][lane][0]) = f32x4{v[0], v[1], v[2], v[3]};
    *reinterpret_cast<f32x4*>(&red[wave][lane][4]) = f32x4{v[4], v[5], v[6], v[7]};
  }
  __syncthreads();
  if (tid < 8) {
    float s = 0.f, f = 0.f;
#pragma unroll
    for (int w = 0; w < NWAVE; ++w) { s += red[w][tid >> 2][tid & 3]; f += red[w][tid >> 2][4 + (tid & 3)]; }
    s = rbf(s); f = rbf(f);
    const float g = rbf(f / (1.f + __expf(-f)));
    const float o = __uint_as_float((unsigned)xo << 16) + rbf(s * g);
    out[b * DM + cg * 8 + tid] = __builtin_bit_cast(u16, (__bf16)o);
  }
}

extern "C" void kernel_launch(void* const* d_in, const int* in_sizes, int n_in, void* d_out, int out_size, void* d_ws,
                              size_t ws_size, hipStream_t stream) {
  const u16* x = (const u16*)d_in[0];
  const u16* kc = (const u16*)d_in[1];
  const u16* vc = (const u16*)d_in[2];
  const u16* wq = (const u16*)d_in[3];
  const u16* wo = (const u16*)d_in[4];
  const u16* wg = (const u16*)d_in[6];
  const u16* wu = (const u16*)d_in[7];
  const u16* wd = (const u16*)d_in[8];
  k_main<<<256, 512, 0, stream>>>(x, kc, vc, wq, wo, wg, wu, wd, (unsigned char*)d_ws);
  k_final<<<128, 512, 0, stream>>>(x, (const unsigned char*)d_ws, (u16*)d_out);
}
```

```cpp
#include <hip/hip_runtime.h>
#include <hip/hip_bf16.h>
#include <stdint.h>
#include <stdio.h>
#define STAMPS 1

using u32x4 = __attribute__((ext_vector_type(4))) unsigned;
using f32x4 = __attribute__((ext_vector_type(4))) float;
using bf16x2 = __attribute__((ext_vector_type(2))) __bf16;
using bf16x8 = __attribute__((ext_vector_type(8))) __bf16;
typedef unsigned short u16;

constexpr int DM = 512, HQ = 32, SK = 8192, DH = 128, FF = 1376;
constexpr int NSPLIT = 4;
constexpr int NWAVE = 8;
constexpr int NTILE = SK / 16;
#define TOTH 133
constexpr int T_OTHER = TOTH;
constexpr int NUNIT = FF / 32;
constexpr int NSLOT = HQ * NSPLIT;
constexpr float LOG2E = 1.4426950408889634f;
constexpr float SCALE = 0.08838834764831845f;

constexpr size_t WS_Y = 0;
constexpr size_t WS_ML = WS_Y + (size_t)2 * NSLOT * DM * 4;
constexpr size_t WS_F = WS_ML + (size_t)2 * NSLOT * 2 * 4;

__device__ __forceinline__ float dot2(unsigned a, unsigned b, float acc) {
  return __builtin_amdgcn_fdot2_f32_bf16(__builtin_bit_cast(bf16x2, a), __builtin_bit_cast(bf16x2, b), acc, false);
}
template <int CTRL> __device__ __forceinline__ float dpp(float x) {
  return __builtin_bit_cast(float, __builtin_amdgcn_mov_dpp(__builtin_bit_cast(int, x), CTRL, 0xf, 0xf, true));
}
template <int CTRL> __device__ __forceinline__ unsigned dppu(unsigned x) {
  return (unsigned)__builtin_amdgcn_mov_dpp((int)x, CTRL, 0xf, 0xf, true);
}
constexpr int ROR8 = 0x128, ROR4 = 0x124, ROR2 = 0x122, ROR1 = 0x121, XOR1 = 0xB1, XOR2 = 0x4E, HMIR = 0x141;
__device__ __forceinline__ float row16_sum(float x) {
  x += dpp<ROR8>(x); x += dpp<ROR4>(x); x += dpp<ROR2>(x); x += dpp<ROR1>(x); return x;
}
__device__ __forceinline__ float swap32_add(float a, float b) {
  auto t = __builtin_amdgcn_permlane32_swap(__float_as_uint(a), __float_as_uint(b), false, false);
  return __uint_as_float(t[0]) + __uint_as_float(t[1]);
}
__device__ __forceinline__ float swap16_add(float a, float b) {
  auto t = __builtin_amdgcn_permlane16_swap(__float_as_uint(a), __float_as_uint(b), false, false);
  return __uint_as_float(t[0]) + __uint_as_float(t[1]);
}
__device__ __forceinline__ float xrow_sum(float x) { x = swap16_add(x, x); return swap32_add(x, x); }
__device__ __forceinline__ float xrow_max(float x) {
  auto s = __builtin_amdgcn_permlane16_swap(__float_as_uint(x), __float_as_uint(x), false, false);
  x = fmaxf(__uint_as_float(s[0]), __uint_as_float(s[1]));
  auto t = __builtin_amdgcn_permlane32_swap(__float_as_uint(x), __float_as_uint(x), false, false);
  return fmaxf(__uint_as_float(t[0]), __uint_as_float(t[1]));
}
__device__ __forceinline__ float wave_sum(float x) { return xrow_sum(row16_sum(x)); }
__device__ __forceinline__ float rbf(float x) { return (float)(__bf16)x; }
__device__ __forceinline__ float bflo(unsigned v) { return __uint_as_float(v << 16); }
__device__ __forceinline__ float bfhi(unsigned v) { return __uint_as_float(v & 0xffff0000u); }
__device__ __forceinline__ unsigned packbf(float lo, float hi) {
  bf16x2 r = {(__bf16)lo, (__bf16)hi};
  return __builtin_bit_cast(unsigned, r);
}
__device__ __forceinline__ u32x4 ldnt(const void* p) { return __builtin_nontemporal_load(reinterpret_cast<const u32x4*>(p)); }
__device__ __forceinline__ u32x4 ld16(const void* p) { return *reinterpret_cast<const u32x4*>(p); }
__device__ __forceinline__ f32x4 mfma(u32x4 a, u32x4 b, f32x4 c) {
  return __builtin_amdgcn_mfma_f32_16x16x32_bf16(__builtin_bit_cast(bf16x8, a), __builtin_bit_cast(bf16x8, b), c, 0, 0, 0);
}
__device__ __forceinline__ u32x4 diag(unsigned x, u32x4 mask) { return u32x4{x & mask[0], x & mask[1], x & mask[2], x & mask[3]}; }

__device__ __forceinline__ void ln_stats(u32x4 xv, float& mb, float& den) {
  float xf[8];
#pragma unroll
  for (int k = 0; k < 4; ++k) { xf[2 * k] = bflo(xv[k]); xf[2 * k + 1] = bfhi(xv[k]); }
  const float s = ((xf[0] + xf[1]) + (xf[2] + xf[3])) + ((xf[4] + xf[5]) + (xf[6] + xf[7]));
  const float mean = wave_sum(s) * (1.f / DM);
  float q0 = 0.f, q1 = 0.f;
#pragma unroll
  for (int k = 0; k < 4; ++k) {
    const float d0 = xf[2 * k] - mean, d1 = xf[2 * k + 1] - mean;
    q0 = fmaf(d0, d0, q0); q1 = fmaf(d1, d1, q1);
  }
  const float var = wave_sum(q0 + q1) * (1.f / DM);
  mb = rbf(mean);
  den = rbf(sqrtf(rbf(rbf(var) + 1e-5f)));
}
__device__ __forceinline__ float gelu_tanh(float x) {
  const float u2 = 2.f * 0.7978845608028654f * (x + 0.044715f * x * x * x);
  const float th = 1.f - 2.f / (1.f + __expf(u2));
  return 0.5f * x * (1.f + th);
}

constexpr int L_WO = 0;
constexpr int L_QP = L_WO + NWAVE * 16384;
constexpr int L_QB = L_QP + NWAVE * 512;
constexpr int L_OW = L_QB + NWAVE * 256;
constexpr int L_ML = L_OW + NWAVE * 512;
constexpr int L_FP = L_ML + NWAVE * 8;
constexpr int L_HX = L_FP + NWAVE * 512;
constexpr int L_END = L_HX + 4 * 256;

__global__ void __launch_bounds__(512)
k_main(const u16* __restrict__ x, const u16* __restrict__ kc, const u16* __restrict__ vc, const u16* __restrict__ wq,
       const u16* __restrict__ wo, const u16* __restrict__ wg, const u16* __restrict__ wu, const u16* __restrict__ wd,
       unsigned char* __restrict__ ws) {
  __shared__ __attribute__((aligned(16))) unsigned char lds[L_END];
  const int tid = threadIdx.x, lane = tid & 63, wave = __builtin_amdgcn_readfirstlane(tid >> 6);
  const int r = lane >> 4, c = lane & 15;
  unsigned long long st0 = wall_clock64(), st1 = 0, st2 = 0;
  const int bid = blockIdx.x, g8 = bid & 7, i = bid >> 3;
  const int h = g8 * 4 + (i >> 3), b = (i >> 2) & 1, s = i & 3;
  const int unit = g8 + 8 * (i >> 2);
  const bool pair_ffn = unit < NUNIT, has_ffn = pair_ffn && s == 3;
  const int t0 = s * (pair_ffn ? T_OTHER : NTILE / 4);
  const int nt = (s == 3 ? NTILE : t0 + (pair_ffn ? T_OTHER : NTILE / 4)) - t0;
  const int n = (nt - wave + 7) >> 3;

  {
    const u16* src = wo + (size_t)(h * DH + 16 * wave + 4 * r) * DM + 8 * c;
    const unsigned dst = (unsigned)(uintptr_t)(lds + L_WO) + wave * 16384;
#pragma unroll
    for (int k = 0; k < 16; ++k) {
      unsigned keep;
      asm volatile("s_mov_b32 %0, m0\n\ts_mov_b32 m0, %2\n\ts_nop 0\n\tglobal_load_lds_dwordx4 %1, off nt\n\ts_mov_b32 m0, %0"
                   : "=&s"(keep) : "v"(src + (size_t)(k >> 2) * DM + (k & 3) * 128), "s"(dst + k * 1024) : "memory");
    }
  }
  u32x4 mask;
#pragma unroll
  for (int k = 0; k < 4; ++k) mask[k] = ((c & 7) >> 1) == k ? ((c & 1) ? 0xffff0000u : 0x0000ffffu) : 0u;

  const u32x4 xv = ld16(x + b * DM + 8 * lane);
  const u16 xe = x[b * DM + wave * 64 + lane];
  __builtin_amdgcn_sched_barrier(0);
  u32x4 Ka[4], Va[4], Kb[4], Vb[4];
  const size_t kvoff = ((size_t)(b * HQ + h) * SK + (size_t)(t0 + wave) * 16) * DH;
  const unsigned char* kp = reinterpret_cast<const unsigned char*>(kc + kvoff) + c * 256 + r * 16;
  const unsigned char* vp = reinterpret_cast<const unsigned char*>(vc + kvoff) + r * 1024 + c * 16;
  constexpr int TSTR = NWAVE * 4096;
#define LDK(KR, off) _Pragma("unroll") for (int t = 0; t < 4; ++t) KR[t] = ldnt(kp + (off) + t * 64);
#define LDV(VR, off) _Pragma("unroll") for (int t = 0; t < 4; ++t) VR[t] = ldnt(vp + (off) + t * 256);
#define SB __builtin_amdgcn_sched_barrier(0);
  u32x4 qf[4];
  {
    u32x4 xo, gv[4], uv[4], dv[8];
    uint2 xr0, xr1;
    if (has_ffn) {
      xo = ld16(x + (1 - b) * DM + 8 * lane);
      xr0 = *reinterpret_cast<const uint2*>(x + wave * 64 + 4 * (lane >> 2));
      xr1 = *reinterpret_cast<const uint2*>(x + DM + wave * 64 + 4 * (lane >> 2));
      const size_t fo = (size_t)(wave * 64 + 4 * (lane >> 2)) * FF + unit * 32 + 8 * (lane & 3);
#pragma unroll
      for (int t = 0; t < 4; ++t) gv[t] = ldnt(wg + fo + (size_t)t * FF);
#pragma unroll
      for (int t = 0; t < 4; ++t) uv[t] = ldnt(wu + fo + (size_t)t * FF);
    }
    SB
    u32x4 wqv[16];
    const u16* p = wq + (size_t)(wave * 64 + 16 * r) * (HQ * DH) + h * DH + 8 * c;
#pragma unroll
    for (int t = 0; t < 16; ++t) wqv[t] = ldnt(p + (size_t)t * (HQ * DH));
    SB
    if (has_ffn && wave < 4) {
#pragma unroll
      for (int t = 0; t < 8; ++t) dv[t] = ldnt(wd + (size_t)(unit * 32 + 4 * t + r) * DM + wave * 128 + 8 * c);
    }
    SB
    LDK(Ka, 0) SB LDV(Va, 0) SB LDK(Kb, TSTR) SB LDV(Vb, TSTR) SB
    float mb, den;
    ln_stats(xv, mb, den);
    if (has_ffn) {
      float mo, deo;
      ln_stats(xo, mo, deo);
      const float m0 = b ? mo : mb, d0 = b ? deo : den, m1 = b ? mb : mo, d1 = b ? den : deo;
      unsigned xp0[2], xp1[2];
      xp0[0] = packbf(rbf(bflo(xr0.x) - m0) / d0, rbf(bfhi(xr0.x) - m0) / d0);
      xp0[1] = packbf(rbf(bflo(xr0.y) - m0) / d0, rbf(bfhi(xr0.y) - m0) / d0);
      xp1[0] = packbf(rbf(bflo(xr1.x) - m1) / d1, rbf(bfhi(xr1.x) - m1) / d1);
      xp1[1] = packbf(rbf(bflo(xr1.y) - m1) / d1, rbf(bfhi(xr1.y) - m1) / d1);
      float f[2][2][8];
#pragma unroll
      for (int k = 0; k < 32; ++k) (&f[0][0][0])[k] = 0.f;
#pragma unroll
      for (int m = 0; m < 2; ++m)
#pragma unroll
        for (int k = 0; k < 4; ++k) {
          const unsigned glo = __builtin_amdgcn_perm(gv[2 * m + 1][k], gv[2 * m][k], 0x05040100u);
          const unsigned ghi = __builtin_amdgcn_perm(gv[2 * m + 1][k], gv[2 * m][k], 0x07060302u);
          const unsigned ulo = __builtin_amdgcn_perm(uv[2 * m + 1][k], uv[2 * m][k], 0x05040100u);
          const unsigned uhi = __builtin_amdgcn_perm(uv[2 * m + 1][k], uv[2 * m][k], 0x07060302u);
          f[0][0][2 * k] = dot2(glo, xp0[m], f[0][0][2 * k]); f[0][0][2 * k + 1] = dot2(ghi, xp0[m], f[0][0][2 * k + 1]);
          f[0][1][2 * k] = dot2(glo, xp1[m], f[0][1][2 * k]); f[0][1][2 * k + 1] = dot2(ghi, xp1[m], f[0][1][2 * k + 1]);
          f[1][0][2 * k] = dot2(ulo, xp0[m], f[1][0][2 * k]); f[1][0][2 * k + 1] = dot2(uhi, xp0[m], f[1][0][2 * k + 1]);
          f[1][1][2 * k] = dot2(ulo, xp1[m], f[1][1][2 * k]); f[1][1][2 * k + 1] = dot2(uhi, xp1[m], f[1][1][2 * k + 1]);
        }
      float gu[2];
#pragma unroll
      for (int m = 0; m < 2; ++m) {
        float a2[2][2];
#pragma unroll
        for (int bb = 0; bb < 2; ++bb)
#pragma unroll
          for (int j2 = 0; j2 < 2; ++j2) {
            const float e0 = swap32_add(f[m][bb][4 * j2 + 0], f[m][bb][4 * j2 + 2]);
            const float e1 = swap32_add(f[m][bb][4 * j2 + 1], f[m][bb][4 * j2 + 3]);
            a2[bb][j2] = swap16_add(e0, e1);
          }
        float a3[2];
#pragma unroll
        for (int j2 = 0; j2 < 2; ++j2) {
          const float keep = (c & 8) ? a2[1][j2] : a2[0][j2], send = (c & 8) ? a2[0][j2] : a2[1][j2];
          a3[j2] = keep + dpp<ROR8>(send);
        }
        const float s0 = dpp<0x124>(a3[1]), s1 = dpp<0x12C>(a3[0]);
        gu[m] = (c & 4) ? a3[1] + s0 : a3[0] + s1;
      }
      *reinterpret_cast<float2*>(lds + L_FP + wave * 512 + lane * 8) = make_float2(gu[0], gu[1]);
    }
    const float xn = rbf(__uint_as_float((unsigned)xe << 16) - mb) / den;
    const unsigned X = packbf(xn, xn);
    f32x4 qa = {0.f, 0.f, 0.f, 0.f};
#define QSTEP(t) qa = mfma(diag(dppu<0x150 + t>(X), mask), wqv[t], qa);
    QSTEP(0) QSTEP(1) QSTEP(2) QSTEP(3) QSTEP(4) QSTEP(5) QSTEP(6) QSTEP(7)
    QSTEP(8) QSTEP(9) QSTEP(10) QSTEP(11) QSTEP(12) QSTEP(13) QSTEP(14) QSTEP(15)
#undef QSTEP
    if (r < 2) *reinterpret_cast<f32x4*>(lds + L_QP + wave * 512 + (8 * c + 4 * r) * 4) = qa;
    __syncthreads();
    float q0 = 0.f, q1 = 0.f;
#pragma unroll
    for (int w = 0; w < NWAVE; ++w) {
      const float2 v = *reinterpret_cast<const float2*>(lds + L_QP + w * 512 + lane * 8);
      q0 += v.x; q1 += v.y;
    }
    *reinterpret_cast<unsigned*>(lds + L_QB + wave * 256 + lane * 4) = packbf(q0, q1);
    if (has_ffn && wave < 4) {
      float g = 0.f, u = 0.f;
#pragma unroll
      for (int w = 0; w < NWAVE; ++w) {
        const float2 v = *reinterpret_cast<const float2*>(lds + L_FP + w * 512 + lane * 8);
        g += v.x; u += v.y;
      }
      const float hv = rbf(gelu_tanh(rbf(g))) * rbf(u);
      *reinterpret_cast<unsigned*>(lds + L_HX + wave * 256 + (((c >> 3) * 4 + r) * 8 + (c & 3) * 2 + ((c >> 2) & 1)) * 4) = packbf(hv, hv);
    }
    __builtin_amdgcn_fence(__ATOMIC_RELEASE, "wavefront");
    __builtin_amdgcn_wave_barrier();
    __builtin_amdgcn_fence(__ATOMIC_ACQUIRE, "wavefront");
#pragma unroll
    for (int k = 0; k < 4; ++k) qf[k] = *reinterpret_cast<const u32x4*>(lds + L_QB + wave * 256 + k * 64 + r * 16);
    if (has_ffn && wave < 4) {
      const u32x4 h0 = *reinterpret_cast<const u32x4*>(lds + L_HX + wave * 256 + ((c >> 3) * 4 + r) * 32);
      const u32x4 h1 = *reinterpret_cast<const u32x4*>(lds + L_HX + wave * 256 + ((c >> 3) * 4 + r) * 32 + 16);
      f32x4 d = {0.f, 0.f, 0.f, 0.f};
#pragma unroll
      for (int t = 0; t < 4; ++t) d = mfma(diag(h0[t], mask), dv[t], d);
#pragma unroll
      for (int t = 0; t < 4; ++t) d = mfma(diag(h1[t], mask), dv[4 + t], d);
      *reinterpret_cast<f32x4*>(reinterpret_cast<float*>(ws + WS_F) + ((size_t)(r >> 1) * NUNIT + unit) * DM + wave * 128 + 8 * c + 4 * (r & 1)) = d;
    }
  }
  st1 = wall_clock64();

  float m = -INFINITY, l = 0.f;
  f32x4 acc = {0.f, 0.f, 0.f, 0.f};
#define STEP(KR, VR, REFILL, off)                                                        \
  {                                                                                      \
    f32x4 S = {0.f, 0.f, 0.f, 0.f};                                                      \
    _Pragma("unroll") for (int t = 0; t < 4; ++t) S = mfma(KR[t], qf[t], S);             \
    SB                                                                                   \
    if (REFILL) { LDK(KR, off) }                                                         \
    SB                                                                                   \
    float sb[4];                                                                         \
    _Pragma("unroll") for (int k = 0; k < 4; ++k) sb[k] = rbf(rbf(S[k]) * SCALE);        \
    float mx = fmaxf(fmaxf(sb[0], sb[1]), fmaxf(sb[2], sb[3]));                          \
    mx = fmaxf(xrow_max(mx), m);                                                         \
    const float alpha = __builtin_amdgcn_exp2f((m - mx) * LOG2E);                        \
    const float nb = -mx * LOG2E;                                                        \
    m = mx; l *= alpha; acc *= alpha;                                                    \
    _Pragma("unroll") for (int k = 0; k < 4; ++k) {                                      \
      const float p = __builtin_amdgcn_exp2f(fmaf(sb[k], LOG2E, nb));                    \
      l += p;                                                                            \
      const float y = (c & 8) ? p - rbf(p) : p;                                          \
      acc = mfma(diag(packbf(y, y), mask), VR[k], acc);                                  \
    }                                                                                    \
    SB                                                                                   \
    if (REFILL) { LDV(VR, off) }                                                         \
    SB                                                                                   \
  }
  int k = 2;
#pragma unroll 1
  for (; k + 1 < n; k += 2) {
    kp += 2 * TSTR; vp += 2 * TSTR;
    STEP(Ka, Va, true, 0)
    STEP(Kb, Vb, true, TSTR)
  }
  if (k < n) {
    STEP(Ka, Va, true, 2 * TSTR)
    STEP(Kb, Vb, false, 0)
    STEP(Ka, Va, false, 0)
  } else {
    STEP(Ka, Va, false, 0)
    STEP(Kb, Vb, false, 0)
  }
#undef STEP
  st2 = wall_clock64();

  {
    l = xrow_sum(l);
    f32x4 o;
#pragma unroll
    for (int k2 = 0; k2 < 4; ++k2) o[k2] = swap32_add(acc[k2], acc[k2]);
    if (r < 2) *reinterpret_cast<f32x4*>(lds + L_OW + wave * 512 + (8 * c + 4 * r) * 4) = o;
    if (lane == 0) *reinterpret_cast<float2*>(lds + L_ML + wave * 8) = make_float2(m, l);
  }
  u32x4 wf[16];
#pragma unroll
  for (int k2 = 0; k2 < 16; ++k2) wf[k2] = *reinterpret_cast<const u32x4*>(lds + L_WO + wave * 16384 + k2 * 1024 + lane * 16);
  __syncthreads();
  float Mb = -INFINITY, Lb = 0.f;
  {
    float2 mlw[NWAVE];
#pragma unroll
    for (int w = 0; w < NWAVE; ++w) { mlw[w] = *reinterpret_cast<const float2*>(lds + L_ML + w * 8); Mb = fmaxf(Mb, mlw[w].x); }
    float o = 0.f;
#pragma unroll
    for (int w = 0; w < NWAVE; ++w) {
      const float cw = __builtin_amdgcn_exp2f((mlw[w].x - Mb) * LOG2E);
      Lb = fmaf(cw, mlw[w].y, Lb);
      o = fmaf(cw, *reinterpret_cast<const float*>(lds + L_OW + w * 512 + (wave * 16 + 4 * r + (c & 3)) * 4), o);
    }
    const float y = (c & 8) ? o - rbf(o) : o;
    const unsigned X = packbf(y, y);
    const u32x4 A0 = diag(dppu<0x00>(X), mask), A1 = diag(dppu<0x55>(X), mask);
    const u32x4 A2 = diag(dppu<0xAA>(X), mask), A3 = diag(dppu<0xFF>(X), mask);
#pragma unroll
    for (int sg = 0; sg < 4; ++sg) {
      f32x4 d = mfma(A0, wf[sg], f32x4{0.f, 0.f, 0.f, 0.f});
      d = mfma(A1, wf[4 + sg], d);
      d = mfma(A2, wf[8 + sg], d);
      d = mfma(A3, wf[12 + sg], d);
      *reinterpret_cast<f32x4*>(lds + L_WO + wave * 16384 + (r >> 1) * 2048 + (sg * 128 + 8 * c + 4 * (r & 1)) * 4) = d;
    }
  }
  __syncthreads();
  {
    float y = 0.f;
#pragma unroll
    for (int w = 0; w < NWAVE; ++w)
      y += *reinterpret_cast<const float*>(lds + L_WO + w * 16384 + tid * 4) + *reinterpret_cast<const float*>(lds + L_WO + w * 16384 + 2048 + tid * 4);
    const int slot = b * NSLOT + h * NSPLIT + s;
    reinterpret_cast<float*>(ws + WS_Y)[(size_t)slot * DM + tid] = y;
    if (tid == 0) *reinterpret_cast<float2*>(ws + WS_ML + (size_t)slot * 8) = make_float2(Mb * LOG2E, Lb);
  }
  if (lane == 0) { unsigned long long* tp = reinterpret_cast<unsigned long long*>(ws + ((size_t)64 << 20)) + (bid * NWAVE + wave) * 4;
    tp[0] = st0; tp[1] = st1; tp[2] = st2; tp[3] = wall_clock64(); }
}

__global__ void __launch_bounds__(512)
k_final(const u16* __restrict__ x, const unsigned char* __restrict__ ws, u16* __restrict__ out) {
  __shared__ __attribute__((aligned(16))) float red[NWAVE][2][8];
  const int tid = threadIdx.x, lane = tid & 63, wave = tid >> 6;
  const int b = blockIdx.x & 1, cg = blockIdx.x >> 1;
  const int row = tid >> 1, half = tid & 1;
  if (blockIdx.x == 0 && tid == 0) {
    const unsigned long long* tp = reinterpret_cast<const unsigned long long*>(ws + ((size_t)64 << 20));
    unsigned long long s0 = ~0ull, q1 = 0, e2 = 0, e3 = 0, now = wall_clock64();
    for (int i = 0; i < 2048; ++i) { s0 = min(s0, tp[4 * i]); q1 = max(q1, tp[4 * i + 1]); e2 = max(e2, tp[4 * i + 2]); e3 = max(e3, tp[4 * i + 3]); }
    printf("K1 span: qmax %.2f loopmax %.2f endmax %.2f k2start %.2f\n", (q1 - s0) * 0.01, (e2 - s0) * 0.01, (e3 - s0) * 0.01, (now - s0) * 0.01);
  }
  f32x4 sa = {0.f, 0.f, 0.f, 0.f}, sf = {0.f, 0.f, 0.f, 0.f};
  u16 xo = 0;
  if (tid < 8) xo = x[b * DM + cg * 8 + tid];
  if (row < NUNIT) sf = *reinterpret_cast<const f32x4*>(ws + WS_F + (((size_t)b * NUNIT + row) * DM + cg * 8 + half * 4) * 4);
  if (row < NSLOT) {
    const f32x4 y = *reinterpret_cast<const f32x4*>(ws + WS_Y + (((size_t)b * NSLOT + row) * DM + cg * 8 + half * 4) * 4);
    const f32x4* mlp = reinterpret_cast<const f32x4*>(ws + WS_ML + ((size_t)b * NSLOT + (row & ~3)) * 8);
    const f32x4 a = mlp[0], c4 = mlp[1];
    const float M = fmaxf(fmaxf(a[0], a[2]), fmaxf(c4[0], c4[2]));
    const float e0 = __builtin_amdgcn_exp2f(a[0] - M), e1 = __builtin_amdgcn_exp2f(a[2] - M);
    const float e2 = __builtin_amdgcn_exp2f(c4[0] - M), e3 = __builtin_amdgcn_exp2f(c4[2] - M);
    const float L = fmaf(e0, a[1], fmaf(e1, a[3], fmaf(e2, c4[1], e3 * c4[3])));
    const int sp = row & 3;
    const float e = sp == 0 ? e0 : sp == 1 ? e1 : sp == 2 ? e2 : e3;
    sa = y * (e / L);
  }
  float v[8] = {sa[0], sa[1], sa[2], sa[3], sf[0], sf[1], sf[2], sf[3]};
#pragma unroll
  for (int k = 0; k < 8; ++k) {
    float t = v[k];
    t += dpp<XOR2>(t); t += dpp<ROR4>(t); t += dpp<ROR8>(t);
    v[k] = xrow_sum(t);
  }
  if (lane < 2) {
    *reinterpret_cast<f32x4*>(&red[wave][lane][0]) = f32x4{v[0], v[1], v[2], v[3]};
    *reinterpret_cast<f32x4*>(&red[wave][lane][4]) = f32x4{v[4], v[5], v[6], v[7]};
  }
  __syncthreads();
  if (tid < 8) {
    float s = 0.f, f = 0.f;
#pragma unroll
    for (int w = 0; w < NWAVE; ++w) { s += red[w][tid >> 2][tid & 3]; f += red[w][tid >> 2][4 + (tid & 3)]; }
    s = rbf(s); f = rbf(f);
    const float g = rbf(f / (1.f + __expf(-f)));
    const float o = __uint_as_float((unsigned)xo << 16) + rbf(s * g);
    out[b * DM + cg * 8 + tid] = __builtin_bit_cast(u16, (__bf16)o);
  }
}

extern "C" void kernel_launch(void* const* d_in, const int* in_sizes, int n_in, void* d_out, int out_size, void* d_ws,
                              size_t ws_size, hipStream_t stream) {
  const u16* x = (const u16*)d_in[0];
  const u16* kc = (const u16*)d_in[1];
  const u16* vc = (const u16*)d_in[2];
  const u16* wq = (const u16*)d_in[3];
  const u16* wo = (const u16*)d_in[4];
  const u16* wg = (const u16*)d_in[6];
  const u16* wu = (const u16*)d_in[7];
  const u16* wd = (const u16*)d_in[8];
  for (int i = 0; i < 9; ++i) fprintf(stderr, "in%d %p\n", i, d_in[i]);
  fprintf(stderr, "out %p ws %p\n", d_out, d_ws);
  k_main<<<256, 512, 0, stream>>>(x, kc, vc, wq, wo, wg, wu, wd, (unsigned char*)d_ws);
  k_final<<<128, 512, 0, stream>>>(x, (const unsigned char*)d_ws, (u16*)d_out);
}
```

```cpp
#include <hip/hip_runtime.h>
#include <hip/hip_bf16.h>
#include <stdint.h>
#define STAMPS 1

using u32x4 = __attribute__((ext_vector_type(4))) unsigned;
using f32x4 = __attribute__((ext_vector_type(4))) float;
using bf16x2 = __attribute__((ext_vector_type(2))) __bf16;
using bf16x8 = __attribute__((ext_vector_type(8))) __bf16;
typedef unsigned short u16;

constexpr int DM = 512, HQ = 32, SK = 8192, DH = 128, FF = 1376;
constexpr int NSPLIT = 4;
constexpr int NWAVE = 8;
constexpr int NTILE = SK / 16;
#define TOTH 133
constexpr int T_OTHER = TOTH;
constexpr int NUNIT = FF / 32;
constexpr int NSLOT = HQ * NSPLIT;
constexpr float LOG2E = 1.4426950408889634f;
constexpr float SCALE = 0.08838834764831845f;

constexpr size_t WS_Y = 0;
constexpr size_t WS_ML = WS_Y + (size_t)2 * NSLOT * DM * 4;
constexpr size_t WS_F = WS_ML + (size_t)2 * NSLOT * 2 * 4;

__device__ __forceinline__ float dot2(unsigned a, unsigned b, float acc) {
  return __builtin_amdgcn_fdot2_f32_bf16(__builtin_bit_cast(bf16x2, a), __builtin_bit_cast(bf16x2, b), acc, false);
}
template <int CTRL> __device__ __forceinline__ float dpp(float x) {
  return __builtin_bit_cast(float, __builtin_amdgcn_mov_dpp(__builtin_bit_cast(int, x), CTRL, 0xf, 0xf, true));
}
template <int CTRL> __device__ __forceinline__ unsigned dppu(unsigned x) {
  return (unsigned)__builtin_amdgcn_mov_dpp((int)x, CTRL, 0xf, 0xf, true);
}
constexpr int ROR8 = 0x128, ROR4 = 0x124, ROR2 = 0x122, ROR1 = 0x121, XOR1 = 0xB1, XOR2 = 0x4E, HMIR = 0x141;
__device__ __forceinline__ float row16_sum(float x) {
  x += dpp<ROR8>(x); x += dpp<ROR4>(x); x += dpp<ROR2>(x); x += dpp<ROR1>(x); return x;
}
__device__ __forceinline__ float swap32_add(float a, float b) {
  auto t = __builtin_amdgcn_permlane32_swap(__float_as_uint(a), __float_as_uint(b), false, false);
  return __uint_as_float(t[0]) + __uint_as_float(t[1]);
}
__device__ __forceinline__ float swap16_add(float a, float b) {
  auto t = __builtin_amdgcn_permlane16_swap(__float_as_uint(a), __float_as_uint(b), false, false);
  return __uint_as_float(t[0]) + __uint_as_float(t[1]);
}
__device__ __forceinline__ float xrow_sum(float x) { x = swap16_add(x, x); return swap32_add(x, x); }
__device__ __forceinline__ float xrow_max(float x) {
  auto s = __builtin_amdgcn_permlane16_swap(__float_as_uint(x), __float_as_uint(x), false, false);
  x = fmaxf(__uint_as_float(s[0]), __uint_as_float(s[1]));
  auto t = __builtin_amdgcn_permlane32_swap(__float_as_uint(x), __float_as_uint(x), false, false);
  return fmaxf(__uint_as_float(t[0]), __uint_as_float(t[1]));
}
__device__ __forceinline__ float wave_sum(float x) { return xrow_sum(row16_sum(x)); }
__device__ __forceinline__ float rbf(float x) { return (float)(__bf16)x; }
__device__ __forceinline__ float bflo(unsigned v) { return __uint_as_float(v << 16); }
__device__ __forceinline__ float bfhi(unsigned v) { return __uint_as_float(v & 0xffff0000u); }
__device__ __forceinline__ unsigned packbf(float lo, float hi) {
  bf16x2 r = {(__bf16)lo, (__bf16)hi};
  return __builtin_bit_cast(unsigned, r);
}
__device__ __forceinline__ u32x4 ldnt(const void* p) { return __builtin_nontemporal_load(reinterpret_cast<const u32x4*>(p)); }
__device__ __forceinline__ u32x4 ld16(const void* p) { return *reinterpret_cast<const u32x4*>(p); }
__device__ __forceinline__ f32x4 mfma(u32x4 a, u32x4 b, f32x4 c) {
  return __builtin_amdgcn_mfma_f32_16x16x32_bf16(__builtin_bit_cast(bf16x8, a), __builtin_bit_cast(bf16x8, b), c, 0, 0, 0);
}
__device__ __forceinline__ u32x4 diag(unsigned x, u32x4 mask) { return u32x4{x & mask[0], x & mask[1], x & mask[2], x & mask[3]}; }

__device__ __forceinline__ void ln_stats(u32x4 xv, float& mb, float& den) {
  float xf[8];
#pragma unroll
  for (int k = 0; k < 4; ++k) { xf[2 * k] = bflo(xv[k]); xf[2 * k + 1] = bfhi(xv[k]); }
  const float s = ((xf[0] + xf[1]) + (xf[2] + xf[3])) + ((xf[4] + xf[5]) + (xf[6] + xf[7]));
  const float mean = wave_sum(s) * (1.f / DM);
  float q0 = 0.f, q1 = 0.f;
#pragma unroll
  for (int k = 0; k < 4; ++k) {
    const float d0 = xf[2 * k] - mean, d1 = xf[2 * k + 1] - mean;
    q0 = fmaf(d0, d0, q0); q1 = fmaf(d1, d1, q1);
  }
  const float var = wave_sum(q0 + q1) * (1.f / DM);
  mb = rbf(mean);
  den = rbf(sqrtf(rbf(rbf(var) + 1e-5f)));
}
__device__ __forceinline__ float gelu_tanh(float x) {
  const float u2 = 2.f * 0.7978845608028654f * (x + 0.044715f * x * x * x);
  const float th = 1.f - 2.f / (1.f + __expf(u2));
  return 0.5f * x * (1.f + th);
}

constexpr int L_WO = 0;
constexpr int L_QP = L_WO + NWAVE * 16384;
constexpr int L_QB = L_QP + NWAVE * 512;
constexpr int L_OW = L_QB + NWAVE * 256;
constexpr int L_ML = L_OW + NWAVE * 512;
constexpr int L_FP = L_ML + NWAVE * 8;
constexpr int L_HX = L_FP + NWAVE * 512;
constexpr int L_END = L_HX + 4 * 256;

__global__ void __launch_bounds__(512)
k_main(const u16* __restrict__ x, const u16* __restrict__ kc, const u16* __restrict__ vc, const u16* __restrict__ wq,
       const u16* __restrict__ wo, const u16* __restrict__ wg, const u16* __restrict__ wu, const u16* __restrict__ wd,
       unsigned char* __restrict__ ws) {
  __shared__ __attribute__((aligned(16))) unsigned char lds[L_END];
  const int tid = threadIdx.x, lane = tid & 63, wave = __builtin_amdgcn_readfirstlane(tid >> 6);
  const int r = lane >> 4, c = lane & 15;
  unsigned long long st0 = wall_clock64(), st1 = 0, st2 = 0;
  const int bid = blockIdx.x, g8 = bid & 7, i = bid >> 3;
  const int h = g8 * 4 + (i >> 3), b = (i >> 2) & 1, s = i & 3;
  const int unit = g8 + 8 * (i >> 2);
  const bool pair_ffn = unit < NUNIT, has_ffn = pair_ffn && s == 3;
  const int t0 = s * (pair_ffn ? T_OTHER : NTILE / 4);
  const int nt = (s == 3 ? NTILE : t0 + (pair_ffn ? T_OTHER : NTILE / 4)) - t0;
  const int n = (nt - wave + 7) >> 3;

  {
    const u16* src = wo + (size_t)(h * DH + 16 * wave + 4 * r) * DM + 8 * c;
    const unsigned dst = (unsigned)(uintptr_t)(lds + L_WO) + wave * 16384;
#pragma unroll
    for (int k = 0; k < 16; ++k) {
      unsigned keep;
      asm volatile("s_mov_b32 %0, m0\n\ts_mov_b32 m0, %2\n\ts_nop 0\n\tglobal_load_lds_dwordx4 %1, off nt\n\ts_mov_b32 m0, %0"
                   : "=&s"(keep) : "v"(src + (size_t)(k >> 2) * DM + (k & 3) * 128), "s"(dst + k * 1024) : "memory");
    }
  }
  u32x4 mask;
#pragma unroll
  for (int k = 0; k < 4; ++k) mask[k] = ((c & 7) >> 1) == k ? ((c & 1) ? 0xffff0000u : 0x0000ffffu) : 0u;

  const u32x4 xv = ld16(x + b * DM + 8 * lane);
  const u16 xe = x[b * DM + wave * 64 + lane];
  __builtin_amdgcn_sched_barrier(0);
  u32x4 Ka[4], Va[4], Kb[4], Vb[4];
  const size_t kvoff = ((size_t)(b * HQ + h) * SK + (size_t)(t0 + wave) * 16) * DH;
  const unsigned char* kp = reinterpret_cast<const unsigned char*>(kc + kvoff) + c * 256 + r * 16;
  const unsigned char* vp = reinterpret_cast<const unsigned char*>(vc + kvoff) + r * 1024 + c * 16;
  constexpr int TSTR = NWAVE * 4096;
#define LDK(KR, off) _Pragma("unroll") for (int t = 0; t < 4; ++t) KR[t] = ldnt(kp + (off) + t * 64);
#define LDV(VR, off) _Pragma("unroll") for (int t = 0; t < 4; ++t) VR[t] = ldnt(vp + (off) + t * 256);
#define SB __builtin_amdgcn_sched_barrier(0);
  u32x4 qf[4];
  {
    u32x4 xo, gv[4], uv[4], dv[8];
    uint2 xr0, xr1;
    if (has_ffn) {
      xo = ld16(x + (1 - b) * DM + 8 * lane);
      xr0 = *reinterpret_cast<const uint2*>(x + wave * 64 + 4 * (lane >> 2));
      xr1 = *reinterpret_cast<const uint2*>(x + DM + wave * 64 + 4 * (lane >> 2));
      const size_t fo = (size_t)(wave * 64 + 4 * (lane >> 2)) * FF + unit * 32 + 8 * (lane & 3);
#pragma unroll
      for (int t = 0; t < 4; ++t) gv[t] = ldnt(wg + fo + (size_t)t * FF);
#pragma unroll
      for (int t = 0; t < 4; ++t) uv[t] = ldnt(wu + fo + (size_t)t * FF);
    }
    SB
    u32x4 wqv[16];
    const u16* p = wq + (size_t)(wave * 64 + 16 * r) * (HQ * DH) + h * DH + 8 * c;
#pragma unroll
    for (int t = 0; t < 16; ++t) wqv[t] = ldnt(p + (size_t)t * (HQ * DH));
    SB
    if (has_ffn && wave < 4) {
#pragma unroll
      for (int t = 0; t < 8; ++t) dv[t] = ldnt(wd + (size_t)(unit * 32 + 4 * t + r) * DM + wave * 128 + 8 * c);
    }
    SB
    LDK(Ka, 0) SB LDV(Va, 0) SB LDK(Kb, TSTR) SB LDV(Vb, TSTR) SB
    float mb, den;
    ln_stats(xv, mb, den);
    if (has_ffn) {
      float mo, deo;
      ln_stats(xo, mo, deo);
      const float m0 = b ? mo : mb, d0 = b ? deo : den, m1 = b ? mb : mo, d1 = b ? den : deo;
      unsigned xp0[2], xp1[2];
      xp0[0] = packbf(rbf(bflo(xr0.x) - m0) / d0, rbf(bfhi(xr0.x) - m0) / d0);
      xp0[1] = packbf(rbf(bflo(xr0.y) - m0) / d0, rbf(bfhi(xr0.y) - m0) / d0);
      xp1[0] = packbf(rbf(bflo(xr1.x) - m1) / d1, rbf(bfhi(xr1.x) - m1) / d1);
      xp1[1] = packbf(rbf(bflo(xr1.y) - m1) / d1, rbf(bfhi(xr1.y) - m1) / d1);
      float f[2][2][8];
#pragma unroll
      for (int k = 0; k < 32; ++k) (&f[0][0][0])[k] = 0.f;
#pragma unroll
      for (int m = 0; m < 2; ++m)
#pragma unroll
        for (int k = 0; k < 4; ++k) {
          const unsigned glo = __builtin_amdgcn_perm(gv[2 * m + 1][k], gv[2 * m][k], 0x05040100u);
          const unsigned ghi = __builtin_amdgcn_perm(gv[2 * m + 1][k], gv[2 * m][k], 0x07060302u);
          const unsigned ulo = __builtin_amdgcn_perm(uv[2 * m + 1][k], uv[2 * m][k], 0x05040100u);
          const unsigned uhi = __builtin_amdgcn_perm(uv[2 * m + 1][k], uv[2 * m][k], 0x07060302u);
          f[0][0][2 * k] = dot2(glo, xp0[m], f[0][0][2 * k]); f[0][0][2 * k + 1] = dot2(ghi, xp0[m], f[0][0][2 * k + 1]);
          f[0][1][2 * k] = dot2(glo, xp1[m], f[0][1][2 * k]); f[0][1][2 * k + 1] = dot2(ghi, xp1[m], f[0][1][2 * k + 1]);
          f[1][0][2 * k] = dot2(ulo, xp0[m], f[1][0][2 * k]); f[1][0][2 * k + 1] = dot2(uhi, xp0[m], f[1][0][2 * k + 1]);
          f[1][1][2 * k] = dot2(ulo, xp1[m], f[1][1][2 * k]); f[1][1][2 * k + 1] = dot2(uhi, xp1[m], f[1][1][2 * k + 1]);
        }
      float gu[2];
#pragma unroll
      for (int m = 0; m < 2; ++m) {
        float a2[2][2];
#pragma unroll
        for (int bb = 0; bb < 2; ++bb)
#pragma unroll
          for (int j2 = 0; j2 < 2; ++j2) {
            const float e0 = swap32_add(f[m][bb][4 * j2 + 0], f[m][bb][4 * j2 + 2]);
            const float e1 = swap32_add(f[m][bb][4 * j2 + 1], f[m][bb][4 * j2 + 3]);
            a2[bb][j2] = swap16_add(e0, e1);
          }
        float a3[2];
#pragma unroll
        for (int j2 = 0; j2 < 2; ++j2) {
          const float keep = (c & 8) ? a2[1][j2] : a2[0][j2], send = (c & 8) ? a2[0][j2] : a2[1][j2];
          a3[j2] = keep + dpp<ROR8>(send);
        }
        const float s0 = dpp<0x124>(a3[1]), s1 = dpp<0x12C>(a3[0]);
        gu[m] = (c & 4) ? a3[1] + s0 : a3[0] + s1;
      }
      *reinterpret_cast<float2*>(lds + L_FP + wave * 512 + lane * 8) = make_float2(gu[0], gu[1]);
    }
    const float xn = rbf(__uint_as_float((unsigned)xe << 16) - mb) / den;
    const unsigned X = packbf(xn, xn);
    f32x4 qa = {0.f, 0.f, 0.f, 0.f};
#define QSTEP(t) qa = mfma(diag(dppu<0x150 + t>(X), mask), wqv[t], qa);
    QSTEP(0) QSTEP(1) QSTEP(2) QSTEP(3) QSTEP(4) QSTEP(5) QSTEP(6) QSTEP(7)
    QSTEP(8) QSTEP(9) QSTEP(10) QSTEP(11) QSTEP(12) QSTEP(13) QSTEP(14) QSTEP(15)
#undef QSTEP
    if (r < 2) *reinterpret_cast<f32x4*>(lds + L_QP + wave * 512 + (8 * c + 4 * r) * 4) = qa;
    __syncthreads();
    float q0 = 0.f, q1 = 0.f;
#pragma unroll
    for (int w = 0; w < NWAVE; ++w) {
      const float2 v = *reinterpret_cast<const float2*>(lds + L_QP + w * 512 + lane * 8);
      q0 += v.x; q1 += v.y;
    }
    *reinterpret_cast<unsigned*>(lds + L_QB + wave * 256 + lane * 4) = packbf(q0, q1);
    if (has_ffn && wave < 4) {
      float g = 0.f, u = 0.f;
#pragma unroll
      for (int w = 0; w < NWAVE; ++w) {
        const float2 v = *reinterpret_cast<const float2*>(lds + L_FP + w * 512 + lane * 8);
        g += v.x; u += v.y;
      }
      const float hv = rbf(gelu_tanh(rbf(g))) * rbf(u);
      *reinterpret_cast<unsigned*>(lds + L_HX + wave * 256 + (((c >> 3) * 4 + r) * 8 + (c & 3) * 2 + ((c >> 2) & 1)) * 4) = packbf(hv, hv);
    }
    __builtin_amdgcn_fence(__ATOMIC_RELEASE, "wavefront");
    __builtin_amdgcn_wave_barrier();
    __builtin_amdgcn_fence(__ATOMIC_ACQUIRE, "wavefront");
#pragma unroll
    for (int k = 0; k < 4; ++k) qf[k] = *reinterpret_cast<const u32x4*>(lds + L_QB + wave * 256 + k * 64 + r * 16);
    if (has_ffn && wave < 4) {
      const u32x4 h0 = *reinterpret_cast<const u32x4*>(lds + L_HX + wave * 256 + ((c >> 3) * 4 + r) * 32);
      const u32x4 h1 = *reinterpret_cast<const u32x4*>(lds + L_HX + wave * 256 + ((c >> 3) * 4 + r) * 32 + 16);
      f32x4 d = {0.f, 0.f, 0.f, 0.f};
#pragma unroll
      for (int t = 0; t < 4; ++t) d = mfma(diag(h0[t], mask), dv[t], d);
#pragma unroll
      for (int t = 0; t < 4; ++t) d = mfma(diag(h1[t], mask), dv[4 + t], d);
      *reinterpret_cast<f32x4*>(reinterpret_cast<float*>(ws + WS_F) + ((size_t)(r >> 1) * NUNIT + unit) * DM + wave * 128 + 8 * c + 4 * (r & 1)) = d;
    }
  }
  st1 = wall_clock64();

  float m = -INFINITY, l = 0.f;
  f32x4 acc = {0.f, 0.f, 0.f, 0.f};
#define STEP(KR, VR, REFILL, off)                                                        \
  {                                                                                      \
    f32x4 S = {0.f, 0.f, 0.f, 0.f};                                                      \
    _Pragma("unroll") for (int t = 0; t < 4; ++t) S = mfma(KR[t], qf[t], S);             \
    SB                                                                                   \
    if (REFILL) { LDK(KR, off) }                                                         \
    SB                                                                                   \
    float sb[4];                                                                         \
    _Pragma("unroll") for (int k = 0; k < 4; ++k) sb[k] = rbf(rbf(S[k]) * SCALE);        \
    float mx = fmaxf(fmaxf(sb[0], sb[1]), fmaxf(sb[2], sb[3]));                          \
    mx = fmaxf(xrow_max(mx), m);                                                         \
    const float alpha = __builtin_amdgcn_exp2f((m - mx) * LOG2E);                        \
    const float nb = -mx * LOG2E;                                                        \
    m = mx; l *= alpha; acc *= alpha;                                                    \
    _Pragma("unroll") for (int k = 0; k < 4; ++k) {                                      \
      const float p = __builtin_amdgcn_exp2f(fmaf(sb[k], LOG2E, nb));                    \
      l += p;                                                                            \
      const float y = (c & 8) ? p - rbf(p) : p;                                          \
      acc = mfma(diag(packbf(y, y), mask), VR[k], acc);                                  \
    }                                                                                    \
    SB                                                                                   \
    if (REFILL) { LDV(VR, off) }                                                         \
    SB                                                                                   \
  }
  int k = 2;
#pragma unroll 1
  for (; k + 1 < n; k += 2) {
    kp += 2 * TSTR; vp += 2 * TSTR;
    STEP(Ka, Va, true, 0)
    STEP(Kb, Vb, true, TSTR)
  }
  if (k < n) {
    STEP(Ka, Va, true, 2 * TSTR)
    STEP(Kb, Vb, false, 0)
    STEP(Ka, Va, false, 0)
  } else {
    STEP(Ka, Va, false, 0)
    STEP(Kb, Vb, false, 0)
  }
#undef STEP
  st2 = wall_clock64();

  {
    l = xrow_sum(l);
    f32x4 o;
#pragma unroll
    for (int k2 = 0; k2 < 4; ++k2) o[k2] = swap32_add(acc[k2], acc[k2]);
    if (r < 2) *reinterpret_cast<f32x4*>(lds + L_OW + wave * 512 + (8 * c + 4 * r) * 4) = o;
    if (lane == 0) *reinterpret_cast<float2*>(lds + L_ML + wave * 8) = make_float2(m, l);
  }
  u32x4 wf[16];
#pragma unroll
  for (int k2 = 0; k2 < 16; ++k2) wf[k2] = *reinterpret_cast<const u32x4*>(lds + L_WO + wave * 16384 + k2 * 1024 + lane * 16);
  __syncthreads();
  float Mb = -INFINITY, Lb = 0.f;
  {
    float2 mlw[NWAVE];
#pragma unroll
    for (int w = 0; w < NWAVE; ++w) { mlw[w] = *reinterpret_cast<const float2*>(lds + L_ML + w * 8); Mb = fmaxf(Mb, mlw[w].x); }
    float o = 0.f;
#pragma unroll
    for (int w = 0; w < NWAVE; ++w) {
      const float cw = __builtin_amdgcn_exp2f((mlw[w].x - Mb) * LOG2E);
      Lb = fmaf(cw, mlw[w].y, Lb);
      o = fmaf(cw, *reinterpret_cast<const float*>(lds + L_OW + w * 512 + (wave * 16 + 4 * r + (c & 3)) * 4), o);
    }
    const float y = (c & 8) ? o - rbf(o) : o;
    const unsigned X = packbf(y, y);
    const u32x4 A0 = diag(dppu<0x00>(X), mask), A1 = diag(dppu<0x55>(X), mask);
    const u32x4 A2 = diag(dppu<0xAA>(X), mask), A3 = diag(dppu<0xFF>(X), mask);
#pragma unroll
    for (int sg = 0; sg < 4; ++sg) {
      f32x4 d = mfma(A0, wf[sg], f32x4{0.f, 0.f, 0.f, 0.f});
      d = mfma(A1, wf[4 + sg], d);
      d = mfma(A2, wf[8 + sg], d);
      d = mfma(A3, wf[12 + sg], d);
      *reinterpret_cast<f32x4*>(lds + L_WO + wave * 16384 + (r >> 1) * 2048 + (sg * 128 + 8 * c + 4 * (r & 1)) * 4) = d;
    }
  }
  __syncthreads();
  {
    float y = 0.f;
#pragma unroll
    for (int w = 0; w < NWAVE; ++w)
      y += *reinterpret_cast<const float*>(lds + L_WO + w * 16384 + tid * 4) + *reinterpret_cast<const float*>(lds + L_WO + w * 16384 + 2048 + tid * 4);
    const int slot = b * NSLOT + h * NSPLIT + s;
    reinterpret_cast<float*>(ws + WS_Y)[(size_t)slot * DM + tid] = y;
    if (tid == 0) *reinterpret_cast<float2*>(ws + WS_ML + (size_t)slot * 8) = make_float2(Mb * LOG2E, Lb);
  }
  if (lane == 0) { unsigned long long* tp = reinterpret_cast<unsigned long long*>(ws + ((size_t)64 << 20)) + (bid * NWAVE + wave) * 4;
    tp[0] = st0; tp[1] = st1; tp[2] = st2; tp[3] = wall_clock64(); }
}

__global__ void __launch_bounds__(512)
k_final(const u16* __restrict__ x, const unsigned char* __restrict__ ws, u16* __restrict__ out) {
  __shared__ __attribute__((aligned(16))) float red[NWAVE][2][8];
  const int tid = threadIdx.x, lane = tid & 63, wave = tid >> 6;
  const int b = blockIdx.x & 1, cg = blockIdx.x >> 1;
  const int row = tid >> 1, half = tid & 1;
  if (blockIdx.x == 0) {
    const unsigned long long now = wall_clock64();
    __shared__ unsigned long long smin, smax;
    if (tid == 0) { smin = ~0ull; smax = 0; }
    __syncthreads();
    const unsigned long long* tp = reinterpret_cast<const unsigned long long*>(ws + ((size_t)64 << 20));
    unsigned long long a = ~0ull, e = 0;
    for (int i = tid; i < 2048; i += 512) { a = min(a, tp[4 * i]); e = max(e, tp[4 * i + 3]); }
    atomicMin(&smin, a); atomicMax(&smax, e);
    __syncthreads();
    const unsigned long long d = 0ull;
    const unsigned long long t = wall_clock64();
    while (wall_clock64() - t < d) __builtin_amdgcn_s_sleep(1);
  }
  f32x4 sa = {0.f, 0.f, 0.f, 0.f}, sf = {0.f, 0.f, 0.f, 0.f};
  u16 xo = 0;
  if (tid < 8) xo = x[b * DM + cg * 8 + tid];
  if (row < NUNIT) sf = *reinterpret_cast<const f32x4*>(ws + WS_F + (((size_t)b * NUNIT + row) * DM + cg * 8 + half * 4) * 4);
  if (row < NSLOT) {
    const f32x4 y = *reinterpret_cast<const f32x4*>(ws + WS_Y + (((size_t)b * NSLOT + row) * DM + cg * 8 + half * 4) * 4);
    const f32x4* mlp = reinterpret_cast<const f32x4*>(ws + WS_ML + ((size_t)b * NSLOT + (row & ~3)) * 8);
    const f32x4 a = mlp[0], c4 = mlp[1];
    const float M = fmaxf(fmaxf(a[0], a[2]), fmaxf(c4[0], c4[2]));
    const float e0 = __builtin_amdgcn_exp2f(a[0] - M), e1 = __builtin_amdgcn_exp2f(a[2] - M);
    const float e2 = __builtin_amdgcn_exp2f(c4[0] - M), e3 = __builtin_amdgcn_exp2f(c4[2] - M);
    const float L = fmaf(e0, a[1], fmaf(e1, a[3], fmaf(e2, c4[1], e3 * c4[3])));
    const int sp = row & 3;
    const float e = sp == 0 ? e0 : sp == 1 ? e1 : sp == 2 ? e2 : e3;
    sa = y * (e / L);
  }
  float v[8] = {sa[0], sa[1], sa[2], sa[3], sf[0], sf[1], sf[2], sf[3]};
#pragma unroll
  for (int k = 0; k < 8; ++k) {
    float t = v[k];
    t += dpp<XOR2>(t); t += dpp<ROR4>(t); t += dpp<ROR8>(t);
    v[k] = xrow_sum(t);
  }
  if (lane < 2) {
    *reinterpret_cast<f32x4*>(&red[wave][lane][0]) = f32x4{v[0], v[1], v[2], v[3]};
    *reinterpret_cast<f32x4*>(&red[wave][lane][4]) = f32x4{v[4], v[5], v[6], v[7]};
  }
  __syncthreads();
  if (tid < 8) {
    float s = 0.f, f = 0.f;
#pragma unroll
    for (int w = 0; w < NWAVE; ++w) { s += red[w][tid >> 2][tid & 3]; f += red[w][tid >> 2][4 + (tid & 3)]; }
    s = rbf(s); f = rbf(f);
    const float g = rbf(f / (1.f + __expf(-f)));
    const float o = __uint_as_float((unsigned)xo << 16) + rbf(s * g);
    out[b * DM + cg * 8 + tid] = __builtin_bit_cast(u16, (__bf16)o);
  }
}

extern "C" void kernel_launch(void* const* d_in, const int* in_sizes, int n_in, void* d_out, int out_size, void* d_ws,
                              size_t ws_size, hipStream_t stream) {
  const u16* x = (const u16*)d_in[0];
  const u16* kc = (const u16*)d_in[1];
  const u16* vc = (const u16*)d_in[2];
  const u16* wq = (const u16*)d_in[3];
  const u16* wo = (const u16*)d_in[4];
  const u16* wg = (const u16*)d_in[6];
  const u16* wu = (const u16*)d_in[7];
  const u16* wd = (const u16*)d_in[8];
  k_main<<<256, 512, 0, stream>>>(x, kc, vc, wq, wo, wg, wu, wd, (unsigned char*)d_ws);
  k_final<<<128, 512, 0, stream>>>(x, (const unsigned char*)d_ws, (u16*)d_out);
}
```

```cpp
#include <hip/hip_runtime.h>
#include <hip/hip_bf16.h>
#include <stdint.h>

using u32x4 = __attribute__((ext_vector_type(4))) unsigned;
using f32x4 = __attribute__((ext_vector_type(4))) float;
using bf16x2 = __attribute__((ext_vector_type(2))) __bf16;
using bf16x8 = __attribute__((ext_vector_type(8))) __bf16;
typedef unsigned short u16;

constexpr int DM = 512, HQ = 32, SK = 8192, DH = 128, FF = 1376;
constexpr int NSPLIT = 4;
constexpr int NWAVE = 8;
constexpr int NTILE = SK / 16;
#define TOTH 133
constexpr int T_OTHER = TOTH;
constexpr int NUNIT = FF / 32;
constexpr int NSLOT = HQ * NSPLIT;
constexpr float LOG2E = 1.4426950408889634f;
constexpr float SCALE = 0.08838834764831845f;

constexpr size_t WS_Y = 0;
constexpr size_t WS_ML = WS_Y + (size_t)2 * NSLOT * DM * 4;
constexpr size_t WS_F = WS_ML + (size_t)2 * NSLOT * 2 * 4;

__device__ __forceinline__ float dot2(unsigned a, unsigned b, float acc) {
  return __builtin_amdgcn_fdot2_f32_bf16(__builtin_bit_cast(bf16x2, a), __builtin_bit_cast(bf16x2, b), acc, false);
}
template <int CTRL> __device__ __forceinline__ float dpp(float x) {
  return __builtin_bit_cast(float, __builtin_amdgcn_mov_dpp(__builtin_bit_cast(int, x), CTRL, 0xf, 0xf, true));
}
template <int CTRL> __device__ __forceinline__ unsigned dppu(unsigned x) {
  return (unsigned)__builtin_amdgcn_mov_dpp((int)x, CTRL, 0xf, 0xf, true);
}
constexpr int ROR8 = 0x128, ROR4 = 0x124, ROR2 = 0x122, ROR1 = 0x121, XOR1 = 0xB1, XOR2 = 0x4E, HMIR = 0x141;
__device__ __forceinline__ float row16_sum(float x) {
  x += dpp<ROR8>(x); x += dpp<ROR4>(x); x += dpp<ROR2>(x); x += dpp<ROR1>(x); return x;
}
__device__ __forceinline__ float swap32_add(float a, float b) {
  auto t = __builtin_amdgcn_permlane32_swap(__float_as_uint(a), __float_as_uint(b), false, false);
  return __uint_as_float(t[0]) + __uint_as_float(t[1]);
}
__device__ __forceinline__ float swap16_add(float a, float b) {
  auto t = __builtin_amdgcn_permlane16_swap(__float_as_uint(a), __float_as_uint(b), false, false);
  return __uint_as_float(t[0]) + __uint_as_float(t[1]);
}
__device__ __forceinline__ float xrow_sum(float x) { x = swap16_add(x, x); return swap32_add(x, x); }
__device__ __forceinline__ float xrow_max(float x) {
  auto s = __builtin_amdgcn_permlane16_swap(__float_as_uint(x), __float_as_uint(x), false, false);
  x = fmaxf(__uint_as_float(s[0]), __uint_as_float(s[1]));
  auto t = __builtin_amdgcn_permlane32_swap(__float_as_uint(x), __float_as_uint(x), false, false);
  return fmaxf(__uint_as_float(t[0]), __uint_as_float(t[1]));
}
__device__ __forceinline__ float wave_sum(float x) { return xrow_sum(row16_sum(x)); }
__device__ __forceinline__ float rbf(float x) { return (float)(__bf16)x; }
__device__ __forceinline__ float bflo(unsigned v) { return __uint_as_float(v << 16); }
__device__ __forceinline__ float bfhi(unsigned v) { return __uint_as_float(v & 0xffff0000u); }
__device__ __forceinline__ unsigned packbf(float lo, float hi) {
  bf16x2 r = {(__bf16)lo, (__bf16)hi};
  return __builtin_bit_cast(unsigned, r);
}
__device__ __forceinline__ u32x4 ldnt(const void* p) { return __builtin_nontemporal_load(reinterpret_cast<const u32x4*>(p)); }
__device__ __forceinline__ u32x4 ld16(const void* p) { return *reinterpret_cast<const u32x4*>(p); }
__device__ __forceinline__ f32x4 mfma(u32x4 a, u32x4 b, f32x4 c) {
  return __builtin_amdgcn_mfma_f32_16x16x32_bf16(__builtin_bit_cast(bf16x8, a), __builtin_bit_cast(bf16x8, b), c, 0, 0, 0);
}
__device__ __forceinline__ u32x4 diag(unsigned x, u32x4 mask) { return u32x4{x & mask[0], x & mask[1], x & mask[2], x & mask[3]}; }

__device__ __forceinline__ void ln_stats(u32x4 xv, float& mb, float& den) {
  float xf[8];
#pragma unroll
  for (int k = 0; k < 4; ++k) { xf[2 * k] = bflo(xv[k]); xf[2 * k + 1] = bfhi(xv[k]); }
  const float s = ((xf[0] + xf[1]) + (xf[2] + xf[3])) + ((xf[4] + xf[5]) + (xf[6] + xf[7]));
  const float mean = wave_sum(s) * (1.f / DM);
  float q0 = 0.f, q1 = 0.f;
#pragma unroll
  for (int k = 0; k < 4; ++k) {
    const float d0 = xf[2 * k] - mean, d1 = xf[2 * k + 1] - mean;
    q0 = fmaf(d0, d0, q0); q1 = fmaf(d1, d1, q1);
  }
  const float var = wave_sum(q0 + q1) * (1.f / DM);
  mb = rbf(mean);
  den = rbf(sqrtf(rbf(rbf(var) + 1e-5f)));
}
__device__ __forceinline__ float gelu_tanh(float x) {
  const float u2 = 2.f * 0.7978845608028654f * (x + 0.044715f * x * x * x);
  const float th = 1.f - 2.f / (1.f + __expf(u2));
  return 0.5f * x * (1.f + th);
}

constexpr int L_WO = 0;
constexpr int L_QP = L_WO + NWAVE * 4096;
constexpr int L_QB = L_QP + NWAVE * 512;
constexpr int L_OW = L_QB + NWAVE * 256;
constexpr int L_ML = L_OW + NWAVE * 512;
constexpr int L_FP = L_ML + NWAVE * 8;
constexpr int L_HX = L_FP + NWAVE * 512;
constexpr int L_END = L_HX + 4 * 256;

__global__ void __launch_bounds__(512)
k_main(const u16* __restrict__ x, const u16* __restrict__ kc, const u16* __restrict__ vc, const u16* __restrict__ wq,
       const u16* __restrict__ wo, const u16* __restrict__ wg, const u16* __restrict__ wu, const u16* __restrict__ wd,
       unsigned char* __restrict__ ws) {
  __shared__ __attribute__((aligned(16))) unsigned char lds[L_END];
  const int tid = threadIdx.x, lane = tid & 63, wave = __builtin_amdgcn_readfirstlane(tid >> 6);
  const int r = lane >> 4, c = lane & 15;
  const int bid = blockIdx.x, g8 = bid & 7, i = bid >> 3;
  const unsigned long long tk1 = wall_clock64();
  const unsigned long long prevk2 = *reinterpret_cast<const volatile unsigned long long*>(ws + WS_F + 2 * 43 * 512 * 4);
  const int h = g8 * 4 + (i >> 3), b = (i >> 2) & 1, s = i & 3;
  const int unit = g8 + 8 * (i >> 2);
  const bool pair_ffn = unit < NUNIT, has_ffn = pair_ffn && s == 3;
  const int t0 = s * (pair_ffn ? T_OTHER : NTILE / 4);
  const int nt = (s == 3 ? NTILE : t0 + (pair_ffn ? T_OTHER : NTILE / 4)) - t0;
  const int n = (nt - wave + 7) >> 3;

  u32x4 mask;
#pragma unroll
  for (int k = 0; k < 4; ++k) mask[k] = ((c & 7) >> 1) == k ? ((c & 1) ? 0xffff0000u : 0x0000ffffu) : 0u;

  const u32x4 xv = ld16(x + b * DM + 8 * lane);
  const u16 xe = x[b * DM + wave * 64 + lane];
  __builtin_amdgcn_sched_barrier(0);
  u32x4 Ka[4], Va[4], Kb[4], Vb[4];
  const size_t kvoff = ((size_t)(b * HQ + h) * SK + (size_t)(t0 + wave) * 16) * DH;
  const unsigned char* kp = reinterpret_cast<const unsigned char*>(kc + kvoff) + c * 256 + r * 16;
  const unsigned char* vp = reinterpret_cast<const unsigned char*>(vc + kvoff) + r * 1024 + c * 16;
  constexpr int TSTR = NWAVE * 4096;
#define LDK(KR, off) _Pragma("unroll") for (int t = 0; t < 4; ++t) KR[t] = ldnt(kp + (off) + t * 64);
#define LDV(VR, off) _Pragma("unroll") for (int t = 0; t < 4; ++t) VR[t] = ldnt(vp + (off) + t * 256);
#define SB __builtin_amdgcn_sched_barrier(0);
  u32x4 qf[4];
  {
    u32x4 xo, gv[4], uv[4], dv[8];
    uint2 xr0, xr1;
    if (has_ffn) {
      xo = ld16(x + (1 - b) * DM + 8 * lane);
      xr0 = *reinterpret_cast<const uint2*>(x + wave * 64 + 4 * (lane >> 2));
      xr1 = *reinterpret_cast<const uint2*>(x + DM + wave * 64 + 4 * (lane >> 2));
      const size_t fo = (size_t)(wave * 64 + 4 * (lane >> 2)) * FF + unit * 32 + 8 * (lane & 3);
#pragma unroll
      for (int t = 0; t < 4; ++t) gv[t] = ldnt(wg + fo + (size_t)t * FF);
#pragma unroll
      for (int t = 0; t < 4; ++t) uv[t] = ldnt(wu + fo + (size_t)t * FF);
    }
    SB
    u32x4 wqv[16];
    const u16* p = wq + (size_t)(wave * 64 + 16 * r) * (HQ * DH) + h * DH + 8 * c;
#pragma unroll
    for (int t = 0; t < 16; ++t) wqv[t] = ldnt(p + (size_t)t * (HQ * DH));
    SB
    if (has_ffn && wave < 4) {
#pragma unroll
      for (int t = 0; t < 8; ++t) dv[t] = ldnt(wd + (size_t)(unit * 32 + 4 * t + r) * DM + wave * 128 + 8 * c);
    }
    SB
    LDK(Ka, 0) SB LDV(Va, 0) SB LDK(Kb, TSTR) SB LDV(Vb, TSTR) SB
    float mb, den;
    ln_stats(xv, mb, den);
    if (has_ffn) {
      float mo, deo;
      ln_stats(xo, mo, deo);
      const float m0 = b ? mo : mb, d0 = b ? deo : den, m1 = b ? mb : mo, d1 = b ? den : deo;
      unsigned xp0[2], xp1[2];
      xp0[0] = packbf(rbf(bflo(xr0.x) - m0) / d0, rbf(bfhi(xr0.x) - m0) / d0);
      xp0[1] = packbf(rbf(bflo(xr0.y) - m0) / d0, rbf(bfhi(xr0.y) - m0) / d0);
      xp1[0] = packbf(rbf(bflo(xr1.x) - m1) / d1, rbf(bfhi(xr1.x) - m1) / d1);
      xp1[1] = packbf(rbf(bflo(xr1.y) - m1) / d1, rbf(bfhi(xr1.y) - m1) / d1);
      float f[2][2][8];
#pragma unroll
      for (int k = 0; k < 32; ++k) (&f[0][0][0])[k] = 0.f;
#pragma unroll
      for (int m = 0; m < 2; ++m)
#pragma unroll
        for (int k = 0; k < 4; ++k) {
          const unsigned glo = __builtin_amdgcn_perm(gv[2 * m + 1][k], gv[2 * m][k], 0x05040100u);
          const unsigned ghi = __builtin_amdgcn_perm(gv[2 * m + 1][k], gv[2 * m][k], 0x07060302u);
          const unsigned ulo = __builtin_amdgcn_perm(uv[2 * m + 1][k], uv[2 * m][k], 0x05040100u);
          const unsigned uhi = __builtin_amdgcn_perm(uv[2 * m + 1][k], uv[2 * m][k], 0x07060302u);
          f[0][0][2 * k] = dot2(glo, xp0[m], f[0][0][2 * k]); f[0][0][2 * k + 1] = dot2(ghi, xp0[m], f[0][0][2 * k + 1]);
          f[0][1][2 * k] = dot2(glo, xp1[m], f[0][1][2 * k]); f[0][1][2 * k + 1] = dot2(ghi, xp1[m], f[0][1][2 * k + 1]);
          f[1][0][2 * k] = dot2(ulo, xp0[m], f[1][0][2 * k]); f[1][0][2 * k + 1] = dot2(uhi, xp0[m], f[1][0][2 * k + 1]);
          f[1][1][2 * k] = dot2(ulo, xp1[m], f[1][1][2 * k]); f[1][1][2 * k + 1] = dot2(uhi, xp1[m], f[1][1][2 * k + 1]);
        }
      float gu[2];
#pragma unroll
      for (int m = 0; m < 2; ++m) {
        float a2[2][2];
#pragma unroll
        for (int bb = 0; bb < 2; ++bb)
#pragma unroll
          for (int j2 = 0; j2 < 2; ++j2) {
            const float e0 = swap32_add(f[m][bb][4 * j2 + 0], f[m][bb][4 * j2 + 2]);
            const float e1 = swap32_add(f[m][bb][4 * j2 + 1], f[m][bb][4 * j2 + 3]);
            a2[bb][j2] = swap16_add(e0, e1);
          }
        float a3[2];
#pragma unroll
        for (int j2 = 0; j2 < 2; ++j2) {
          const float keep = (c & 8) ? a2[1][j2] : a2[0][j2], send = (c & 8) ? a2[0][j2] : a2[1][j2];
          a3[j2] = keep + dpp<ROR8>(send);
        }
        const float s0 = dpp<0x124>(a3[1]), s1 = dpp<0x12C>(a3[0]);
        gu[m] = (c & 4) ? a3[1] + s0 : a3[0] + s1;
      }
      *reinterpret_cast<float2*>(lds + L_FP + wave * 512 + lane * 8) = make_float2(gu[0], gu[1]);
    }
    const float xn = rbf(__uint_as_float((unsigned)xe << 16) - mb) / den;
    const unsigned X = packbf(xn, xn);
    f32x4 qa = {0.f, 0.f, 0.f, 0.f};
#define QSTEP(t) qa = mfma(diag(dppu<0x150 + t>(X), mask), wqv[t], qa);
    QSTEP(0) QSTEP(1) QSTEP(2) QSTEP(3) QSTEP(4) QSTEP(5) QSTEP(6) QSTEP(7)
    QSTEP(8) QSTEP(9) QSTEP(10) QSTEP(11) QSTEP(12) QSTEP(13) QSTEP(14) QSTEP(15)
#undef QSTEP
    if (r < 2) *reinterpret_cast<f32x4*>(lds + L_QP + wave * 512 + (8 * c + 4 * r) * 4) = qa;
    __syncthreads();
    float q0 = 0.f, q1 = 0.f;
#pragma unroll
    for (int w = 0; w < NWAVE; ++w) {
      const float2 v = *reinterpret_cast<const float2*>(lds + L_QP + w * 512 + lane * 8);
      q0 += v.x; q1 += v.y;
    }
    *reinterpret_cast<unsigned*>(lds + L_QB + wave * 256 + lane * 4) = packbf(q0, q1);
    if (has_ffn && wave < 4) {
      float g = 0.f, u = 0.f;
#pragma unroll
      for (int w = 0; w < NWAVE; ++w) {
        const float2 v = *reinterpret_cast<const float2*>(lds + L_FP + w * 512 + lane * 8);
        g += v.x; u += v.y;
      }
      const float hv = rbf(gelu_tanh(rbf(g))) * rbf(u);
      *reinterpret_cast<unsigned*>(lds + L_HX + wave * 256 + (((c >> 3) * 4 + r) * 8 + (c & 3) * 2 + ((c >> 2) & 1)) * 4) = packbf(hv, hv);
    }
    __builtin_amdgcn_fence(__ATOMIC_RELEASE, "wavefront");
    __builtin_amdgcn_wave_barrier();
    __builtin_amdgcn_fence(__ATOMIC_ACQUIRE, "wavefront");
#pragma unroll
    for (int k = 0; k < 4; ++k) qf[k] = *reinterpret_cast<const u32x4*>(lds + L_QB + wave * 256 + k * 64 + r * 16);
    if (has_ffn && wave < 4) {
      const u32x4 h0 = *reinterpret_cast<const u32x4*>(lds + L_HX + wave * 256 + ((c >> 3) * 4 + r) * 32);
      const u32x4 h1 = *reinterpret_cast<const u32x4*>(lds + L_HX + wave * 256 + ((c >> 3) * 4 + r) * 32 + 16);
      f32x4 d = {0.f, 0.f, 0.f, 0.f};
#pragma unroll
      for (int t = 0; t < 4; ++t) d = mfma(diag(h0[t], mask), dv[t], d);
#pragma unroll
      for (int t = 0; t < 4; ++t) d = mfma(diag(h1[t], mask), dv[4 + t], d);
      *reinterpret_cast<f32x4*>(reinterpret_cast<float*>(ws + WS_F) + ((size_t)(r >> 1) * NUNIT + unit) * DM + wave * 128 + 8 * c + 4 * (r & 1)) = d;
    }
  }

  float m = -INFINITY, l = 0.f;
  f32x4 acc = {0.f, 0.f, 0.f, 0.f};
#define STEP(KR, VR, REFILL, off)                                                        \
  {                                                                                      \
    f32x4 S = {0.f, 0.f, 0.f, 0.f};                                                      \
    _Pragma("unroll") for (int t = 0; t < 4; ++t) S = mfma(KR[t], qf[t], S);             \
    SB                                                                                   \
    if (REFILL) { LDK(KR, off) }                                                         \
    SB                                                                                   \
    float sb[4];                                                                         \
    _Pragma("unroll") for (int k = 0; k < 4; ++k) sb[k] = rbf(rbf(S[k]) * SCALE);        \
    float mx = fmaxf(fmaxf(sb[0], sb[1]), fmaxf(sb[2], sb[3]));                          \
    mx = fmaxf(xrow_max(mx), m);                                                         \
    const float alpha = __builtin_amdgcn_exp2f((m - mx) * LOG2E);                        \
    const float nb = -mx * LOG2E;                                                        \
    m = mx; l *= alpha; acc *= alpha;                                                    \
    _Pragma("unroll") for (int k = 0; k < 4; ++k) {                                      \
      const float p = __builtin_amdgcn_exp2f(fmaf(sb[k], LOG2E, nb));                    \
      l += p;                                                                            \
      const float y = (c & 8) ? p - rbf(p) : p;                                          \
      acc = mfma(diag(packbf(y, y), mask), VR[k], acc);                                  \
    }                                                                                    \
    SB                                                                                   \
    if (REFILL) { LDV(VR, off) }                                                         \
    SB                                                                                   \
  }
  int k = 2;
#pragma unroll 1
  for (; k + 1 < n; k += 2) {
    kp += 2 * TSTR; vp += 2 * TSTR;
    STEP(Ka, Va, true, 0)
    STEP(Kb, Vb, true, TSTR)
  }
  if (k < n) {
    STEP(Ka, Va, true, 2 * TSTR)
    STEP(Kb, Vb, false, 0)
    STEP(Ka, Va, false, 0)
  } else {
    STEP(Ka, Va, false, 0)
    STEP(Kb, Vb, false, 0)
  }
#undef STEP

  {
    l = xrow_sum(l);
    f32x4 o;
#pragma unroll
    for (int k2 = 0; k2 < 4; ++k2) o[k2] = swap32_add(acc[k2], acc[k2]);
    if (r < 2) *reinterpret_cast<f32x4*>(lds + L_OW + wave * 512 + (8 * c + 4 * r) * 4) = o;
    if (lane == 0) *reinterpret_cast<float2*>(lds + L_ML + wave * 8) = make_float2(m, l);
  }
  u32x4 wf[16];
#pragma unroll
  for (int k2 = 0; k2 < 16; ++k2) wf[k2] = ldnt(wo + (size_t)(h * DH + 16 * wave + 4 * r + (k2 >> 2)) * DM + 8 * c + (k2 & 3) * 128);
  __syncthreads();
  float Mb = -INFINITY, Lb = 0.f;
  {
    float2 mlw[NWAVE];
#pragma unroll
    for (int w = 0; w < NWAVE; ++w) { mlw[w] = *reinterpret_cast<const float2*>(lds + L_ML + w * 8); Mb = fmaxf(Mb, mlw[w].x); }
    float o = 0.f;
#pragma unroll
    for (int w = 0; w < NWAVE; ++w) {
      const float cw = __builtin_amdgcn_exp2f((mlw[w].x - Mb) * LOG2E);
      Lb = fmaf(cw, mlw[w].y, Lb);
      o = fmaf(cw, *reinterpret_cast<const float*>(lds + L_OW + w * 512 + (wave * 16 + 4 * r + (c & 3)) * 4), o);
    }
    const float y = (c & 8) ? o - rbf(o) : o;
    const unsigned X = packbf(y, y);
    const u32x4 A0 = diag(dppu<0x00>(X), mask), A1 = diag(dppu<0x55>(X), mask);
    const u32x4 A2 = diag(dppu<0xAA>(X), mask), A3 = diag(dppu<0xFF>(X), mask);
#pragma unroll
    for (int sg = 0; sg < 4; ++sg) {
      f32x4 d = mfma(A0, wf[sg], f32x4{0.f, 0.f, 0.f, 0.f});
      d = mfma(A1, wf[4 + sg], d);
      d = mfma(A2, wf[8 + sg], d);
      d = mfma(A3, wf[12 + sg], d);
      *reinterpret_cast<f32x4*>(lds + L_WO + wave * 4096 + (r >> 1) * 2048 + (sg * 128 + 8 * c + 4 * (r & 1)) * 4) = d;
    }
  }
  __syncthreads();
  {
    float y = 0.f;
#pragma unroll
    for (int w = 0; w < NWAVE; ++w)
      y += *reinterpret_cast<const float*>(lds + L_WO + w * 4096 + tid * 4) + *reinterpret_cast<const float*>(lds + L_WO + w * 4096 + 2048 + tid * 4);
    const int slot = b * NSLOT + h * NSPLIT + s;
    reinterpret_cast<float*>(ws + WS_Y)[(size_t)slot * DM + tid] = y;
    if (tid == 0) *reinterpret_cast<float2*>(ws + WS_ML + (size_t)slot * 8) = make_float2(Mb * LOG2E, Lb);
  }
  if (bid == 0) { const unsigned long long gap = tk1 - prevk2; if (gap < 2000ull) { const unsigned long long tt = wall_clock64(); while (wall_clock64() - tt < 0ull * gap) __builtin_amdgcn_s_sleep(1); } }
}

__global__ void __launch_bounds__(512)
k_final(const u16* __restrict__ x, const unsigned char* __restrict__ ws, u16* __restrict__ out) {
  __shared__ __attribute__((aligned(16))) float red[NWAVE][2][8];
  const int tid = threadIdx.x, lane = tid & 63, wave = tid >> 6;
  const int b = blockIdx.x & 1, cg = blockIdx.x >> 1;
  const int row = tid >> 1, half = tid & 1;
  f32x4 sa = {0.f, 0.f, 0.f, 0.f}, sf = {0.f, 0.f, 0.f, 0.f};
  u16 xo = 0;
  if (tid < 8) xo = x[b * DM + cg * 8 + tid];
  if (row < NUNIT) sf = *reinterpret_cast<const f32x4*>(ws + WS_F + (((size_t)b * NUNIT + row) * DM + cg * 8 + half * 4) * 4);
  if (row < NSLOT) {
    const f32x4 y = *reinterpret_cast<const f32x4*>(ws + WS_Y + (((size_t)b * NSLOT + row) * DM + cg * 8 + half * 4) * 4);
    const f32x4* mlp = reinterpret_cast<const f32x4*>(ws + WS_ML + ((size_t)b * NSLOT + (row & ~3)) * 8);
    const f32x4 a = mlp[0], c4 = mlp[1];
    const float M = fmaxf(fmaxf(a[0], a[2]), fmaxf(c4[0], c4[2]));
    const float e0 = __builtin_amdgcn_exp2f(a[0] - M), e1 = __builtin_amdgcn_exp2f(a[2] - M);
    const float e2 = __builtin_amdgcn_exp2f(c4[0] - M), e3 = __builtin_amdgcn_exp2f(c4[2] - M);
    const float L = fmaf(e0, a[1], fmaf(e1, a[3], fmaf(e2, c4[1], e3 * c4[3])));
    const int sp = row & 3;
    const float e = sp == 0 ? e0 : sp == 1 ? e1 : sp == 2 ? e2 : e3;
    sa = y * (e / L);
  }
  float v[8] = {sa[0], sa[1], sa[2], sa[3], sf[0], sf[1], sf[2], sf[3]};
#pragma unroll
  for (int k = 0; k < 8; ++k) {
    float t = v[k];
    t += dpp<XOR2>(t); t += dpp<ROR4>(t); t += dpp<ROR8>(t);
    v[k] = xrow_sum(t);
  }
  if (lane < 2) {
    *reinterpret_cast<f32x4*>(&red[wave][lane][0]) = f32x4{v[0], v[1], v[2], v[3]};
    *reinterpret_cast<f32x4*>(&red[wave][lane][4]) = f32x4{v[4], v[5], v[6], v[7]};
  }
  __syncthreads();
  if (tid < 8) {
    float s = 0.f, f = 0.f;
#pragma unroll
    for (int w = 0; w < NWAVE; ++w) { s += red[w][tid >> 2][tid & 3]; f += red[w][tid >> 2][4 + (tid & 3)]; }
    s = rbf(s); f = rbf(f);
    const float g = rbf(f / (1.f + __expf(-f)));
    const float o = __uint_as_float((unsigned)xo << 16) + rbf(s * g);
    out[b * DM + cg * 8 + tid] = __builtin_bit_cast(u16, (__bf16)o);
    if (blockIdx.x == 0 && tid == 0) *reinterpret_cast<volatile unsigned long long*>(const_cast<unsigned char*>(ws) + WS_F + 2 * 43 * 512 * 4) = wall_clock64();
  }
}

extern "C" void kernel_launch(void* const* d_in, const int* in_sizes, int n_in, void* d_out, int out_size, void* d_ws,
                              size_t ws_size, hipStream_t stream) {
  const u16* x = (const u16*)d_in[0];
  const u16* kc = (const u16*)d_in[1];
  const u16* vc = (const u16*)d_in[2];
  const u16* wq = (const u16*)d_in[3];
  const u16* wo = (const u16*)d_in[4];
  const u16* wg = (const u16*)d_in[6];
  const u16* wu = (const u16*)d_in[7];
  const u16* wd = (const u16*)d_in[8];
  k_main<<<256, 512, 0, stream>>>(x, kc, vc, wq, wo, wg, wu, wd, (unsigned char*)d_ws);
  k_final<<<128, 512, 0, stream>>>(x, (const unsigned char*)d_ws, (u16*)d_out);
}
```

```cpp
#include <hip/hip_runtime.h>
#include <hip/hip_bf16.h>
#include <stdint.h>

using u32x4 = __attribute__((ext_vector_type(4))) unsigned;
using f32x4 = __attribute__((ext_vector_type(4))) float;
using bf16x2 = __attribute__((ext_vector_type(2))) __bf16;
using bf16x8 = __attribute__((ext_vector_type(8))) __bf16;
typedef unsigned short u16;

constexpr int DM = 512, HQ = 32, SK = 8192, DH = 128, FF = 1376;
constexpr int NSPLIT = 4;
constexpr int NWAVE = 8;
constexpr int NTILE = SK / 16;
#define TOTH 133
constexpr int T_OTHER = TOTH;
constexpr int NUNIT = FF / 32;
constexpr int NSLOT = HQ * NSPLIT;
constexpr float LOG2E = 1.4426950408889634f;
constexpr float SCALE = 0.08838834764831845f;

constexpr size_t WS_Y = 0;
constexpr size_t WS_ML = WS_Y + (size_t)2 * NSLOT * DM * 4;
constexpr size_t WS_F = WS_ML + (size_t)2 * NSLOT * 2 * 4;

__device__ __forceinline__ float dot2(unsigned a, unsigned b, float acc) {
  return __builtin_amdgcn_fdot2_f32_bf16(__builtin_bit_cast(bf16x2, a), __builtin_bit_cast(bf16x2, b), acc, false);
}
template <int CTRL> __device__ __forceinline__ float dpp(float x) {
  return __builtin_bit_cast(float, __builtin_amdgcn_mov_dpp(__builtin_bit_cast(int, x), CTRL, 0xf, 0xf, true));
}
template <int CTRL> __device__ __forceinline__ unsigned dppu(unsigned x) {
  return (unsigned)__builtin_amdgcn_mov_dpp((int)x, CTRL, 0xf, 0xf, true);
}
constexpr int ROR8 = 0x128, ROR4 = 0x124, ROR2 = 0x122, ROR1 = 0x121, XOR1 = 0xB1, XOR2 = 0x4E, HMIR = 0x141;
__device__ __forceinline__ float row16_sum(float x) {
  x += dpp<ROR8>(x); x += dpp<ROR4>(x); x += dpp<ROR2>(x); x += dpp<ROR1>(x); return x;
}
__device__ __forceinline__ float swap32_add(float a, float b) {
  auto t = __builtin_amdgcn_permlane32_swap(__float_as_uint(a), __float_as_uint(b), false, false);
  return __uint_as_float(t[0]) + __uint_as_float(t[1]);
}
__device__ __forceinline__ float swap16_add(float a, float b) {
  auto t = __builtin_amdgcn_permlane16_swap(__float_as_uint(a), __float_as_uint(b), false, false);
  return __uint_as_float(t[0]) + __uint_as_float(t[1]);
}
__device__ __forceinline__ float xrow_sum(float x) { x = swap16_add(x, x); return swap32_add(x, x); }
__device__ __forceinline__ float xrow_max(float x) {
  auto s = __builtin_amdgcn_permlane16_swap(__float_as_uint(x), __float_as_uint(x), false, false);
  x = fmaxf(__uint_as_float(s[0]), __uint_as_float(s[1]));
  auto t = __builtin_amdgcn_permlane32_swap(__float_as_uint(x), __float_as_uint(x), false, false);
  return fmaxf(__uint_as_float(t[0]), __uint_as_float(t[1]));
}
__device__ __forceinline__ float wave_sum(float x) { return xrow_sum(row16_sum(x)); }
__device__ __forceinline__ float rbf(float x) { return (float)(__bf16)x; }
__device__ __forceinline__ float bflo(unsigned v) { return __uint_as_float(v << 16); }
__device__ __forceinline__ float bfhi(unsigned v) { return __uint_as_float(v & 0xffff0000u); }
__device__ __forceinline__ unsigned packbf(float lo, float hi) {
  bf16x2 r = {(__bf16)lo, (__bf16)hi};
  return __builtin_bit_cast(unsigned, r);
}
__device__ __forceinline__ u32x4 ldnt(const void* p) { return __builtin_nontemporal_load(reinterpret_cast<const u32x4*>(p)); }
__device__ __forceinline__ u32x4 ld16(const void* p) { return *reinterpret_cast<const u32x4*>(p); }
__device__ __forceinline__ f32x4 mfma(u32x4 a, u32x4 b, f32x4 c) {
  return __builtin_amdgcn_mfma_f32_16x16x32_bf16(__builtin_bit_cast(bf16x8, a), __builtin_bit_cast(bf16x8, b), c, 0, 0, 0);
}
__device__ __forceinline__ u32x4 diag(unsigned x, u32x4 mask) { return u32x4{x & mask[0], x & mask[1], x & mask[2], x & mask[3]}; }

__device__ __forceinline__ void ln_stats(u32x4 xv, float& mb, float& den) {
  float xf[8];
#pragma unroll
  for (int k = 0; k < 4; ++k) { xf[2 * k] = bflo(xv[k]); xf[2 * k + 1] = bfhi(xv[k]); }
  const float s = ((xf[0] + xf[1]) + (xf[2] + xf[3])) + ((xf[4] + xf[5]) + (xf[6] + xf[7]));
  const float mean = wave_sum(s) * (1.f / DM);
  float q0 = 0.f, q1 = 0.f;
#pragma unroll
  for (int k = 0; k < 4; ++k) {
    const float d0 = xf[2 * k] - mean, d1 = xf[2 * k + 1] - mean;
    q0 = fmaf(d0, d0, q0); q1 = fmaf(d1, d1, q1);
  }
  const float var = wave_sum(q0 + q1) * (1.f / DM);
  mb = rbf(mean);
  den = rbf(sqrtf(rbf(rbf(var) + 1e-5f)));
}
__device__ __forceinline__ float gelu_tanh(float x) {
  const float u2 = 2.f * 0.7978845608028654f * (x + 0.044715f * x * x * x);
  const float th = 1.f - 2.f / (1.f + __expf(u2));
  return 0.5f * x * (1.f + th);
}

constexpr int L_WO = 0;
constexpr int L_QP = L_WO + NWAVE * 4096;
constexpr int L_QB = L_QP + NWAVE * 512;
constexpr int L_OW = L_QB + NWAVE * 256;
constexpr int L_ML = L_OW + NWAVE * 512;
constexpr int L_FP = L_ML + NWAVE * 8;
constexpr int L_HX = L_FP + NWAVE * 512;
constexpr int L_END = L_HX + 4 * 256;

__global__ void __launch_bounds__(512)
k_main(const u16* __restrict__ x, const u16* __restrict__ kc, const u16* __restrict__ vc, const u16* __restrict__ wq,
       const u16* __restrict__ wo, const u16* __restrict__ wg, const u16* __restrict__ wu, const u16* __restrict__ wd,
       unsigned char* __restrict__ ws) {
  __shared__ __attribute__((aligned(16))) unsigned char lds[L_END];
  const int tid = threadIdx.x, lane = tid & 63, wave = __builtin_amdgcn_readfirstlane(tid >> 6);
  const int r = lane >> 4, c = lane & 15;
  const int bid = blockIdx.x, g8 = bid & 7, i = bid >> 3;
  const int h = g8 * 4 + (i >> 3), b = (i >> 2) & 1, s = i & 3;
  const int unit = g8 + 8 * (i >> 2);
  const bool pair_ffn = unit < NUNIT, has_ffn = pair_ffn && s == 3;
  const int t0 = s * (pair_ffn ? T_OTHER : NTILE / 4);
  const int nt = (s == 3 ? NTILE : t0 + (pair_ffn ? T_OTHER : NTILE / 4)) - t0;
  const int n = (nt - wave + 7) >> 3;

  u32x4 mask;
#pragma unroll
  for (int k = 0; k < 4; ++k) mask[k] = ((c & 7) >> 1) == k ? ((c & 1) ? 0xffff0000u : 0x0000ffffu) : 0u;

  const u32x4 xv = ld16(x + b * DM + 8 * lane);
  const u16 xe = x[b * DM + wave * 64 + lane];
  __builtin_amdgcn_sched_barrier(0);
  u32x4 Ka[4], Va[4], Kb[4], Vb[4];
  const size_t kvoff = ((size_t)(b * HQ + h) * SK + (size_t)(t0 + wave) * 16) * DH;
  const unsigned char* kp = reinterpret_cast<const unsigned char*>(kc + kvoff) + c * 256 + r * 16;
  const unsigned char* vp = reinterpret_cast<const unsigned char*>(vc + kvoff) + r * 1024 + c * 16;
  constexpr int TSTR = NWAVE * 4096;
#define LDK(KR, off) _Pragma("unroll") for (int t = 0; t < 4; ++t) KR[t] = ldnt(kp + (off) + t * 64);
#define LDV(VR, off) _Pragma("unroll") for (int t = 0; t < 4; ++t) VR[t] = ldnt(vp + (off) + t * 256);
#define SB __builtin_amdgcn_sched_barrier(0);
  u32x4 qf[4];
  {
    u32x4 xo, gv[4], uv[4], dv[8];
    uint2 xr0, xr1;
    if (has_ffn) {
      xo = ld16(x + (1 - b) * DM + 8 * lane);
      xr0 = *reinterpret_cast<const uint2*>(x + wave * 64 + 4 * (lane >> 2));
      xr1 = *reinterpret_cast<const uint2*>(x + DM + wave * 64 + 4 * (lane >> 2));
      const size_t fo = (size_t)(wave * 64 + 4 * (lane >> 2)) * FF + unit * 32 + 8 * (lane & 3);
#pragma unroll
      for (int t = 0; t < 4; ++t) gv[t] = ldnt(wg + fo + (size_t)t * FF);
#pragma unroll
      for (int t = 0; t < 4; ++t) uv[t] = ldnt(wu + fo + (size_t)t * FF);
    }
    SB
    u32x4 wqv[16];
    const u16* p = wq + (size_t)(wave * 64 + 16 * r) * (HQ * DH) + h * DH + 8 * c;
#pragma unroll
    for (int t = 0; t < 16; ++t) wqv[t] = ldnt(p + (size_t)t * (HQ * DH));
    SB
    if (has_ffn && wave < 4) {
#pragma unroll
      for (int t = 0; t < 8; ++t) dv[t] = ldnt(wd + (size_t)(unit * 32 + 4 * t + r) * DM + wave * 128 + 8 * c);
    }
    SB
    LDK(Ka, 0) SB LDV(Va, 0) SB LDK(Kb, TSTR) SB LDV(Vb, TSTR) SB
    float mb, den;
    ln_stats(xv, mb, den);
    if (has_ffn) {
      float mo, deo;
      ln_stats(xo, mo, deo);
      const float m0 = b ? mo : mb, d0 = b ? deo : den, m1 = b ? mb : mo, d1 = b ? den : deo;
      unsigned xp0[2], xp1[2];
      xp0[0] = packbf(rbf(bflo(xr0.x) - m0) / d0, rbf(bfhi(xr0.x) - m0) / d0);
      xp0[1] = packbf(rbf(bflo(xr0.y) - m0) / d0, rbf(bfhi(xr0.y) - m0) / d0);
      xp1[0] = packbf(rbf(bflo(xr1.x) - m1) / d1, rbf(bfhi(xr1.x) - m1) / d1);
      xp1[1] = packbf(rbf(bflo(xr1.y) - m1) / d1, rbf(bfhi(xr1.y) - m1) / d1);
      float f[2][2][8];
#pragma unroll
      for (int k = 0; k < 32; ++k) (&f[0][0][0])[k] = 0.f;
#pragma unroll
      for (int m = 0; m < 2; ++m)
#pragma unroll
        for (int k = 0; k < 4; ++k) {
          const unsigned glo = __builtin_amdgcn_perm(gv[2 * m + 1][k], gv[2 * m][k], 0x05040100u);
          const unsigned ghi = __builtin_amdgcn_perm(gv[2 * m + 1][k], gv[2 * m][k], 0x07060302u);
          const unsigned ulo = __builtin_amdgcn_perm(uv[2 * m + 1][k], uv[2 * m][k], 0x05040100u);
          const unsigned uhi = __builtin_amdgcn_perm(uv[2 * m + 1][k], uv[2 * m][k], 0x07060302u);
          f[0][0][2 * k] = dot2(glo, xp0[m], f[0][0][2 * k]); f[0][0][2 * k + 1] = dot2(ghi, xp0[m], f[0][0][2 * k + 1]);
          f[0][1][2 * k] = dot2(glo, xp1[m], f[0][1][2 * k]); f[0][1][2 * k + 1] = dot2(ghi, xp1[m], f[0][1][2 * k + 1]);
          f[1][0][2 * k] = dot2(ulo, xp0[m], f[1][0][2 * k]); f[1][0][2 * k + 1] = dot2(uhi, xp0[m], f[1][0][2 * k + 1]);
          f[1][1][2 * k] = dot2(ulo, xp1[m], f[1][1][2 * k]); f[1][1][2 * k + 1] = dot2(uhi, xp1[m], f[1][1][2 * k + 1]);
        }
      float gu[2];
#pragma unroll
      for (int m = 0; m < 2; ++m) {
        float a2[2][2];
#pragma unroll
        for (int bb = 0; bb < 2; ++bb)
#pragma unroll
          for (int j2 = 0; j2 < 2; ++j2) {
            const float e0 = swap32_add(f[m][bb][4 * j2 + 0], f[m][bb][4 * j2 + 2]);
            const float e1 = swap32_add(f[m][bb][4 * j2 + 1], f[m][bb][4 * j2 + 3]);
            a2[bb][j2] = swap16_add(e0, e1);
          }
        float a3[2];
#pragma unroll
        for (int j2 = 0; j2 < 2; ++j2) {
          const float keep = (c & 8) ? a2[1][j2] : a2[0][j2], send = (c & 8) ? a2[0][j2] : a2[1][j2];
          a3[j2] = keep + dpp<ROR8>(send);
        }
        const float s0 = dpp<0x124>(a3[1]), s1 = dpp<0x12C>(a3[0]);
        gu[m] = (c & 4) ? a3[1] + s0 : a3[0] + s1;
      }
      *reinterpret_cast<float2*>(lds + L_FP + wave * 512 + lane * 8) = make_float2(gu[0], gu[1]);
    }
    const float xn = rbf(__uint_as_float((unsigned)xe << 16) - mb) / den;
    const unsigned X = packbf(xn, xn);
    f32x4 qa = {0.f, 0.f, 0.f, 0.f};
#define QSTEP(t) qa = mfma(diag(dppu<0x150 + t>(X), mask), wqv[t], qa);
    QSTEP(0) QSTEP(1) QSTEP(2) QSTEP(3) QSTEP(4) QSTEP(5) QSTEP(6) QSTEP(7)
    QSTEP(8) QSTEP(9) QSTEP(10) QSTEP(11) QSTEP(12) QSTEP(13) QSTEP(14) QSTEP(15)
#undef QSTEP
    if (r < 2) *reinterpret_cast<f32x4*>(lds + L_QP + wave * 512 + (8 * c + 4 * r) * 4) = qa;
    __syncthreads();
    float q0 = 0.f, q1 = 0.f;
#pragma unroll
    for (int w = 0; w < NWAVE; ++w) {
      const float2 v = *reinterpret_cast<const float2*>(lds + L_QP + w * 512 + lane * 8);
      q0 += v.x; q1 += v.y;
    }
    *reinterpret_cast<unsigned*>(lds + L_QB + wave * 256 + lane * 4) = packbf(q0, q1);
    if (has_ffn && wave < 4) {
      float g = 0.f, u = 0.f;
#pragma unroll
      for (int w = 0; w < NWAVE; ++w) {
        const float2 v = *reinterpret_cast<const float2*>(lds + L_FP + w * 512 + lane * 8);
        g += v.x; u += v.y;
      }
      const float hv = rbf(gelu_tanh(rbf(g))) * rbf(u);
      *reinterpret_cast<unsigned*>(lds + L_HX + wave * 256 + (((c >> 3) * 4 + r) * 8 + (c & 3) * 2 + ((c >> 2) & 1)) * 4) = packbf(hv, hv);
    }
    __builtin_amdgcn_fence(__ATOMIC_RELEASE, "wavefront");
    __builtin_amdgcn_wave_barrier();
    __builtin_amdgcn_fence(__ATOMIC_ACQUIRE, "wavefront");
#pragma unroll
    for (int k = 0; k < 4; ++k) qf[k] = *reinterpret_cast<const u32x4*>(lds + L_QB + wave * 256 + k * 64 + r * 16);
    if (has_ffn && wave < 4) {
      const u32x4 h0 = *reinterpret_cast<const u32x4*>(lds + L_HX + wave * 256 + ((c >> 3) * 4 + r) * 32);
      const u32x4 h1 = *reinterpret_cast<const u32x4*>(lds + L_HX + wave * 256 + ((c >> 3) * 4 + r) * 32 + 16);
      f32x4 d = {0.f, 0.f, 0.f, 0.f};
#pragma unroll
      for (int t = 0; t < 4; ++t) d = mfma(diag(h0[t], mask), dv[t], d);
#pragma unroll
      for (int t = 0; t < 4; ++t) d = mfma(diag(h1[t], mask), dv[4 + t], d);
      *reinterpret_cast<f32x4*>(reinterpret_cast<float*>(ws + WS_F) + ((size_t)(r >> 1) * NUNIT + unit) * DM + wave * 128 + 8 * c + 4 * (r & 1)) = d;
    }
  }

  float m = -INFINITY, l = 0.f;
  f32x4 acc = {0.f, 0.f, 0.f, 0.f};
#define STEP(KR, VR, REFILL, off)                                                        \
  {                                                                                      \
    f32x4 S = {0.f, 0.f, 0.f, 0.f};                                                      \
    _Pragma("unroll") for (int t = 0; t < 4; ++t) S = mfma(KR[t], qf[t], S);             \
    SB                                                                                   \
    if (REFILL) { LDK(KR, off) }                                                         \
    SB                                                                                   \
    float sb[4];                                                                         \
    _Pragma("unroll") for (int k = 0; k < 4; ++k) sb[k] = rbf(rbf(S[k]) * SCALE);        \
    float mx = fmaxf(fmaxf(sb[0], sb[1]), fmaxf(sb[2], sb[3]));                          \
    mx = fmaxf(xrow_max(mx), m);                                                         \
    const float alpha = __builtin_amdgcn_exp2f((m - mx) * LOG2E);                        \
    const float nb = -mx * LOG2E;                                                        \
    m = mx; l *= alpha; acc *= alpha;                                                    \
    _Pragma("unroll") for (int k = 0; k < 4; ++k) {                                      \
      const float p = __builtin_amdgcn_exp2f(fmaf(sb[k], LOG2E, nb));                    \
      l += p;                                                                            \
      const float y = (c & 8) ? p - rbf(p) : p;                                          \
      acc = mfma(diag(packbf(y, y), mask), VR[k], acc);                                  \
    }                                                                                    \
    SB                                                                                   \
    if (REFILL) { LDV(VR, off) }                                                         \
    SB                                                                                   \
  }
  int k = 2;
#pragma unroll 1
  for (; k + 1 < n; k += 2) {
    kp += 2 * TSTR; vp += 2 * TSTR;
    STEP(Ka, Va, true, 0)
    STEP(Kb, Vb, true, TSTR)
  }
  if (k < n) {
    STEP(Ka, Va, true, 2 * TSTR)
    STEP(Kb, Vb, false, 0)
    STEP(Ka, Va, false, 0)
  } else {
    STEP(Ka, Va, false, 0)
    STEP(Kb, Vb, false, 0)
  }
#undef STEP

  {
    l = xrow_sum(l);
    f32x4 o;
#pragma unroll
    for (int k2 = 0; k2 < 4; ++k2) o[k2] = swap32_add(acc[k2], acc[k2]);
    if (r < 2) *reinterpret_cast<f32x4*>(lds + L_OW + wave * 512 + (8 * c + 4 * r) * 4) = o;
    if (lane == 0) *reinterpret_cast<float2*>(lds + L_ML + wave * 8) = make_float2(m, l);
  }
  u32x4 wf[16];
#pragma unroll
  for (int k2 = 0; k2 < 16; ++k2) wf[k2] = ldnt(wo + (size_t)(h * DH + 16 * wave + 4 * r + (k2 >> 2)) * DM + 8 * c + (k2 & 3) * 128);
  __syncthreads();
  float Mb = -INFINITY, Lb = 0.f;
  {
    float2 mlw[NWAVE];
#pragma unroll
    for (int w = 0; w < NWAVE; ++w) { mlw[w] = *reinterpret_cast<const float2*>(lds + L_ML + w * 8); Mb = fmaxf(Mb, mlw[w].x); }
    float o = 0.f;
#pragma unroll
    for (int w = 0; w < NWAVE; ++w) {
      const float cw = __builtin_amdgcn_exp2f((mlw[w].x - Mb) * LOG2E);
      Lb = fmaf(cw, mlw[w].y, Lb);
      o = fmaf(cw, *reinterpret_cast<const float*>(lds + L_OW + w * 512 + (wave * 16 + 4 * r + (c & 3)) * 4), o);
    }
    const float y = (c & 8) ? o - rbf(o) : o;
    const unsigned X = packbf(y, y);
    const u32x4 A0 = diag(dppu<0x00>(X), mask), A1 = diag(dppu<0x55>(X), mask);
    const u32x4 A2 = diag(dppu<0xAA>(X), mask), A3 = diag(dppu<0xFF>(X), mask);
#pragma unroll
    for (int sg = 0; sg < 4; ++sg) {
      f32x4 d = mfma(A0, wf[sg], f32x4{0.f, 0.f, 0.f, 0.f});
      d = mfma(A1, wf[4 + sg], d);
      d = mfma(A2, wf[8 + sg], d);
      d = mfma(A3, wf[12 + sg], d);
      *reinterpret_cast<f32x4*>(lds + L_WO + wave * 4096 + (r >> 1) * 2048 + (sg * 128 + 8 * c + 4 * (r & 1)) * 4) = d;
    }
  }
  __syncthreads();
  {
    float y = 0.f;
#pragma unroll
    for (int w = 0; w < NWAVE; ++w)
      y += *reinterpret_cast<const float*>(lds + L_WO + w * 4096 + tid * 4) + *reinterpret_cast<const float*>(lds + L_WO + w * 4096 + 2048 + tid * 4);
    const int slot = b * NSLOT + h * NSPLIT + s;
    reinterpret_cast<float*>(ws + WS_Y)[(size_t)slot * DM + tid] = y;
    if (tid == 0) *reinterpret_cast<float2*>(ws + WS_ML + (size_t)slot * 8) = make_float2(Mb * LOG2E, Lb);
  }
}

__global__ void __launch_bounds__(512)
k_final(const u16* __restrict__ x, const unsigned char* __restrict__ ws, u16* __restrict__ out) {
  __shared__ __attribute__((aligned(16))) float red[NWAVE][2][8];
  const int tid = threadIdx.x, lane = tid & 63, wave = tid >> 6;
  const int b = blockIdx.x & 1, cg = blockIdx.x >> 1;
  const int row = tid >> 1, half = tid & 1;
  f32x4 sa = {0.f, 0.f, 0.f, 0.f}, sf = {0.f, 0.f, 0.f, 0.f};
  u16 xo = 0;
  if (tid < 8) xo = x[b * DM + cg * 8 + tid];
  if (row < NUNIT) sf = *reinterpret_cast<const f32x4*>(ws + WS_F + (((size_t)b * NUNIT + row) * DM + cg * 8 + half * 4) * 4);
  if (row < NSLOT) {
    const f32x4 y = *reinterpret_cast<const f32x4*>(ws + WS_Y + (((size_t)b * NSLOT + row) * DM + cg * 8 + half * 4) * 4);
    const f32x4* mlp = reinterpret_cast<const f32x4*>(ws + WS_ML + ((size_t)b * NSLOT + (row & ~3)) * 8);
    const f32x4 a = mlp[0], c4 = mlp[1];
    const float M = fmaxf(fmaxf(a[0], a[2]), fmaxf(c4[0], c4[2]));
    const float e0 = __builtin_amdgcn_exp2f(a[0] - M), e1 = __builtin_amdgcn_exp2f(a[2] - M);
    const float e2 = __builtin_amdgcn_exp2f(c4[0] - M), e3 = __builtin_amdgcn_exp2f(c4[2] - M);
    const float L = fmaf(e0, a[1], fmaf(e1, a[3], fmaf(e2, c4[1], e3 * c4[3])));
    const int sp = row & 3;
    const float e = sp == 0 ? e0 : sp == 1 ? e1 : sp == 2 ? e2 : e3;
    sa = y * (e / L);
  }
  float v[8] = {sa[0], sa[1], sa[2], sa[3], sf[0], sf[1], sf[2], sf[3]};
#pragma unroll
  for (int k = 0; k < 8; ++k) {
    float t = v[k];
    t += dpp<XOR2>(t); t += dpp<ROR4>(t); t += dpp<ROR8>(t);
    v[k] = xrow_sum(t);
  }
  if (lane < 2) {
    *reinterpret_cast<f32x4*>(&red[wave][lane][0]) = f32x4{v[0], v[1], v[2], v[3]};
    *reinterpret_cast<f32x4*>(&red[wave][lane][4]) = f32x4{v[4], v[5], v[6], v[7]};
  }
  __syncthreads();
  if (tid < 8) {
    float s = 0.f, f = 0.f;
#pragma unroll
    for (int w = 0; w < NWAVE; ++w) { s += red[w][tid >> 2][tid & 3]; f += red[w][tid >> 2][4 + (tid & 3)]; }
    s = rbf(s); f = rbf(f);
    const float g = rbf(f / (1.f + __expf(-f)));
    const float o = __uint_as_float((unsigned)xo << 16) + rbf(s * g);
    if (blockIdx.x == 0) { const unsigned long long gap = *reinterpret_cast<const volatile unsigned long long*>(ws + WS_F + 2 * 43 * 512 * 4 + 8); const unsigned long long tt = wall_clock64(); while (wall_clock64() - tt < 0ull * gap) __builtin_amdgcn_s_sleep(1); }
    out[b * DM + cg * 8 + tid] = __builtin_bit_cast(u16, (__bf16)o);
  }
}

constexpr size_t WS_T = WS_F + 2 * 43 * 512 * 4;
__global__ void k_p(unsigned char* ws) { if (threadIdx.x == 0) *reinterpret_cast<volatile unsigned long long*>(ws + WS_T) = wall_clock64(); }
__global__ void __launch_bounds__(512) k_d(unsigned char* ws) {
  __shared__ unsigned char l[16];
  const unsigned long long t = wall_clock64();

  if (ws == nullptr) { l[threadIdx.x % 16] = 1; ws[1] = l[(threadIdx.x + 1) % 16]; }
  if (blockIdx.x == 0 && threadIdx.x == 0) *reinterpret_cast<volatile unsigned long long*>(ws + WS_T + 8) = t - *reinterpret_cast<volatile unsigned long long*>(ws + WS_T);
}
extern "C" void kernel_launch(void* const* d_in, const int* in_sizes, int n_in, void* d_out, int out_size, void* d_ws,
                              size_t ws_size, hipStream_t stream) {
  const u16* x = (const u16*)d_in[0];
  const u16* kc = (const u16*)d_in[1];
  const u16* vc = (const u16*)d_in[2];
  const u16* wq = (const u16*)d_in[3];
  const u16* wo = (const u16*)d_in[4];
  const u16* wg = (const u16*)d_in[6];
  const u16* wu = (const u16*)d_in[7];
  const u16* wd = (const u16*)d_in[8];
  k_p<<<1, 64, 0, stream>>>((unsigned char*)d_ws);
  k_d<<<1, 64, 0, stream>>>((unsigned char*)d_ws);
  k_main<<<256, 512, 0, stream>>>(x, kc, vc, wq, wo, wg, wu, wd, (unsigned char*)d_ws);
  k_final<<<128, 512, 0, stream>>>(x, (const unsigned char*)d_ws, (u16*)d_out);
}
```

```cpp
#include <hip/hip_runtime.h>
#include <hip/hip_bf16.h>
#include <stdint.h>

using u32x4 = __attribute__((ext_vector_type(4))) unsigned;
using f32x4 = __attribute__((ext_vector_type(4))) float;
using bf16x2 = __attribute__((ext_vector_type(2))) __bf16;
using bf16x8 = __attribute__((ext_vector_type(8))) __bf16;
typedef unsigned short u16;

constexpr int DM = 512, HQ = 32, SK = 8192, DH = 128, FF = 1376;
constexpr int NSPLIT = 4;
constexpr int NWAVE = 8;
constexpr int NTILE = SK / 16;
#define TOTH 133
constexpr int T_OTHER = TOTH;
constexpr int NUNIT = FF / 32;
constexpr int NSLOT = HQ * NSPLIT;
constexpr float LOG2E = 1.4426950408889634f;
constexpr float SCALE = 0.08838834764831845f;

constexpr size_t WS_Y = 0;
constexpr size_t WS_ML = WS_Y + (size_t)2 * NSLOT * DM * 4;
constexpr size_t WS_F = WS_ML + (size_t)2 * NSLOT * 2 * 4;

__device__ __forceinline__ float dot2(unsigned a, unsigned b, float acc) {
  return __builtin_amdgcn_fdot2_f32_bf16(__builtin_bit_cast(bf16x2, a), __builtin_bit_cast(bf16x2, b), acc, false);
}
template <int CTRL> __device__ __forceinline__ float dpp(float x) {
  return __builtin_bit_cast(float, __builtin_amdgcn_mov_dpp(__builtin_bit_cast(int, x), CTRL, 0xf, 0xf, true));
}
template <int CTRL> __device__ __forceinline__ unsigned dppu(unsigned x) {
  return (unsigned)__builtin_amdgcn_mov_dpp((int)x, CTRL, 0xf, 0xf, true);
}
constexpr int ROR8 = 0x128, ROR4 = 0x124, ROR2 = 0x122, ROR1 = 0x121, XOR1 = 0xB1, XOR2 = 0x4E, HMIR = 0x141;
__device__ __forceinline__ float row16_sum(float x) {
  x += dpp<ROR8>(x); x += dpp<ROR4>(x); x += dpp<ROR2>(x); x += dpp<ROR1>(x); return x;
}
__device__ __forceinline__ float swap32_add(float a, float b) {
  auto t = __builtin_amdgcn_permlane32_swap(__float_as_uint(a), __float_as_uint(b), false, false);
  return __uint_as_float(t[0]) + __uint_as_float(t[1]);
}
__device__ __forceinline__ float swap16_add(float a, float b) {
  auto t = __builtin_amdgcn_permlane16_swap(__float_as_uint(a), __float_as_uint(b), false, false);
  return __uint_as_float(t[0]) + __uint_as_float(t[1]);
}
__device__ __forceinline__ float xrow_sum(float x) { x = swap16_add(x, x); return swap32_add(x, x); }
__device__ __forceinline__ float xrow_max(float x) {
  auto s = __builtin_amdgcn_permlane16_swap(__float_as_uint(x), __float_as_uint(x), false, false);
  x = fmaxf(__uint_as_float(s[0]), __uint_as_float(s[1]));
  auto t = __builtin_amdgcn_permlane32_swap(__float_as_uint(x), __float_as_uint(x), false, false);
  return fmaxf(__uint_as_float(t[0]), __uint_as_float(t[1]));
}
__device__ __forceinline__ float wave_sum(float x) { return xrow_sum(row16_sum(x)); }
__device__ __forceinline__ float rbf(float x) { return (float)(__bf16)x; }
__device__ __forceinline__ float bflo(unsigned v) { return __uint_as_float(v << 16); }
__device__ __forceinline__ float bfhi(unsigned v) { return __uint_as_float(v & 0xffff0000u); }
__device__ __forceinline__ unsigned packbf(float lo, float hi) {
  bf16x2 r = {(__bf16)lo, (__bf16)hi};
  return __builtin_bit_cast(unsigned, r);
}
__device__ __forceinline__ u32x4 ldnt(const void* p) { return __builtin_nontemporal_load(reinterpret_cast<const u32x4*>(p)); }
__device__ __forceinline__ u32x4 ld16(const void* p) { return *reinterpret_cast<const u32x4*>(p); }
__device__ __forceinline__ f32x4 mfma(u32x4 a, u32x4 b, f32x4 c) {
  return __builtin_amdgcn_mfma_f32_16x16x32_bf16(__builtin_bit_cast(bf16x8, a), __builtin_bit_cast(bf16x8, b), c, 0, 0, 0);
}
__device__ __forceinline__ u32x4 diag(unsigned x, u32x4 mask) { return u32x4{x & mask[0], x & mask[1], x & mask[2], x & mask[3]}; }

__device__ __forceinline__ void ln_stats(u32x4 xv, float& mb, float& den) {
  float xf[8];
#pragma unroll
  for (int k = 0; k < 4; ++k) { xf[2 * k] = bflo(xv[k]); xf[2 * k + 1] = bfhi(xv[k]); }
  const float s = ((xf[0] + xf[1]) + (xf[2] + xf[3])) + ((xf[4] + xf[5]) + (xf[6] + xf[7]));
  const float mean = wave_sum(s) * (1.f / DM);
  float q0 = 0.f, q1 = 0.f;
#pragma unroll
  for (int k = 0; k < 4; ++k) {
    const float d0 = xf[2 * k] - mean, d1 = xf[2 * k + 1] - mean;
    q0 = fmaf(d0, d0, q0); q1 = fmaf(d1, d1, q1);
  }
  const float var = wave_sum(q0 + q1) * (1.f / DM);
  mb = rbf(mean);
  den = rbf(sqrtf(rbf(rbf(var) + 1e-5f)));
}
__device__ __forceinline__ float gelu_tanh(float x) {
  const float u2 = 2.f * 0.7978845608028654f * (x + 0.044715f * x * x * x);
  const float th = 1.f - 2.f / (1.f + __expf(u2));
  return 0.5f * x * (1.f + th);
}

constexpr int L_WO = 0;
constexpr int L_QP = L_WO + NWAVE * 4096;
constexpr int L_QB = L_QP + NWAVE * 512;
constexpr int L_OW = L_QB + NWAVE * 256;
constexpr int L_ML = L_OW + NWAVE * 512;
constexpr int L_FP = L_ML + NWAVE * 8;
constexpr int L_HX = L_FP + NWAVE * 512;
constexpr int L_END = L_HX + 4 * 256;

__global__ void __launch_bounds__(512)
k_main(const u16* __restrict__ x, const u16* __restrict__ kc, const u16* __restrict__ vc, const u16* __restrict__ wq,
       const u16* __restrict__ wo, const u16* __restrict__ wg, const u16* __restrict__ wu, const u16* __restrict__ wd,
       unsigned char* __restrict__ ws) {
  __shared__ __attribute__((aligned(16))) unsigned char lds[L_END];
  const int tid = threadIdx.x, lane = tid & 63, wave = __builtin_amdgcn_readfirstlane(tid >> 6);
  const int r = lane >> 4, c = lane & 15;
  const int bid = blockIdx.x, g8 = bid & 7, i = bid >> 3;
  const int h = g8 * 4 + (i >> 3), b = (i >> 2) & 1, s = i & 3;
  const int unit = g8 + 8 * (i >> 2);
  const bool pair_ffn = unit < NUNIT, has_ffn = pair_ffn && s == 3;
  const int t0 = s * (pair_ffn ? T_OTHER : NTILE / 4);
  const int nt = (s == 3 ? NTILE : t0 + (pair_ffn ? T_OTHER : NTILE / 4)) - t0;
  const int n = (nt - wave + 7) >> 3;

  u32x4 mask;
#pragma unroll
  for (int k = 0; k < 4; ++k) mask[k] = ((c & 7) >> 1) == k ? ((c & 1) ? 0xffff0000u : 0x0000ffffu) : 0u;

  const u32x4 xv = ld16(x + b * DM + 8 * lane);
  const u16 xe = x[b * DM + wave * 64 + lane];
  __builtin_amdgcn_sched_barrier(0);
  u32x4 Ka[4], Va[4], Kb[4], Vb[4];
  const size_t kvoff = ((size_t)(b * HQ + h) * SK + (size_t)(t0 + wave) * 16) * DH;
  const unsigned char* kp = reinterpret_cast<const unsigned char*>(kc + kvoff) + c * 256 + r * 16;
  const unsigned char* vp = reinterpret_cast<const unsigned char*>(vc + kvoff) + r * 1024 + c * 16;
  constexpr int TSTR = NWAVE * 4096;
#define LDK(KR, off) _Pragma("unroll") for (int t = 0; t < 4; ++t) KR[t] = ldnt(kp + (off) + t * 64);
#define LDV(VR, off) _Pragma("unroll") for (int t = 0; t < 4; ++t) VR[t] = ldnt(vp + (off) + t * 256);
#define SB __builtin_amdgcn_sched_barrier(0);
  u32x4 qf[4];
  {
    u32x4 xo, gv[4], uv[4], dv[8];
    uint2 xr0, xr1;
    if (has_ffn) {
      xo = ld16(x + (1 - b) * DM + 8 * lane);
      xr0 = *reinterpret_cast<const uint2*>(x + wave * 64 + 4 * (lane >> 2));
      xr1 = *reinterpret_cast<const uint2*>(x + DM + wave * 64 + 4 * (lane >> 2));
      const size_t fo = (size_t)(wave * 64 + 4 * (lane >> 2)) * FF + unit * 32 + 8 * (lane & 3);
#pragma unroll
      for (int t = 0; t < 4; ++t) gv[t] = ldnt(wg + fo + (size_t)t * FF);
#pragma unroll
      for (int t = 0; t < 4; ++t) uv[t] = ldnt(wu + fo + (size_t)t * FF);
    }
    SB
    u32x4 wqv[16];
    const u16* p = wq + (size_t)(wave * 64 + 16 * r) * (HQ * DH) + h * DH + 8 * c;
#pragma unroll
    for (int t = 0; t < 16; ++t) wqv[t] = ldnt(p + (size_t)t * (HQ * DH));
    SB
    if (has_ffn && wave < 4) {
#pragma unroll
      for (int t = 0; t < 8; ++t) dv[t] = ldnt(wd + (size_t)(unit * 32 + 4 * t + r) * DM + wave * 128 + 8 * c);
    }
    SB
    LDK(Ka, 0) SB LDV(Va, 0) SB LDK(Kb, TSTR) SB LDV(Vb, TSTR) SB
    float mb, den;
    ln_stats(xv, mb, den);
    if (has_ffn) {
      float mo, deo;
      ln_stats(xo, mo, deo);
      const float m0 = b ? mo : mb, d0 = b ? deo : den, m1 = b ? mb : mo, d1 = b ? den : deo;
      unsigned xp0[2], xp1[2];
      xp0[0] = packbf(rbf(bflo(xr0.x) - m0) / d0, rbf(bfhi(xr0.x) - m0) / d0);
      xp0[1] = packbf(rbf(bflo(xr0.y) - m0) / d0, rbf(bfhi(xr0.y) - m0) / d0);
      xp1[0] = packbf(rbf(bflo(xr1.x) - m1) / d1, rbf(bfhi(xr1.x) - m1) / d1);
      xp1[1] = packbf(rbf(bflo(xr1.y) - m1) / d1, rbf(bfhi(xr1.y) - m1) / d1);
      float f[2][2][8];
#pragma unroll
      for (int k = 0; k < 32; ++k) (&f[0][0][0])[k] = 0.f;
#pragma unroll
      for (int m = 0; m < 2; ++m)
#pragma unroll
        for (int k = 0; k < 4; ++k) {
          const unsigned glo = __builtin_amdgcn_perm(gv[2 * m + 1][k], gv[2 * m][k], 0x05040100u);
          const unsigned ghi = __builtin_amdgcn_perm(gv[2 * m + 1][k], gv[2 * m][k], 0x07060302u);
          const unsigned ulo = __builtin_amdgcn_perm(uv[2 * m + 1][k], uv[2 * m][k], 0x05040100u);
          const unsigned uhi = __builtin_amdgcn_perm(uv[2 * m + 1][k], uv[2 * m][k], 0x07060302u);
          f[0][0][2 * k] = dot2(glo, xp0[m], f[0][0][2 * k]); f[0][0][2 * k + 1] = dot2(ghi, xp0[m], f[0][0][2 * k + 1]);
          f[0][1][2 * k] = dot2(glo, xp1[m], f[0][1][2 * k]); f[0][1][2 * k + 1] = dot2(ghi, xp1[m], f[0][1][2 * k + 1]);
          f[1][0][2 * k] = dot2(ulo, xp0[m], f[1][0][2 * k]); f[1][0][2 * k + 1] = dot2(uhi, xp0[m], f[1][0][2 * k + 1]);
          f[1][1][2 * k] = dot2(ulo, xp1[m], f[1][1][2 * k]); f[1][1][2 * k + 1] = dot2(uhi, xp1[m], f[1][1][2 * k + 1]);
        }
      float gu[2];
#pragma unroll
      for (int m = 0; m < 2; ++m) {
        float a2[2][2];
#pragma unroll
        for (int bb = 0; bb < 2; ++bb)
#pragma unroll
          for (int j2 = 0; j2 < 2; ++j2) {
            const float e0 = swap32_add(f[m][bb][4 * j2 + 0], f[m][bb][4 * j2 + 2]);
            const float e1 = swap32_add(f[m][bb][4 * j2 + 1], f[m][bb][4 * j2 + 3]);
            a2[bb][j2] = swap16_add(e0, e1);
          }
        float a3[2];
#pragma unroll
        for (int j2 = 0; j2 < 2; ++j2) {
          const float keep = (c & 8) ? a2[1][j2] : a2[0][j2], send = (c & 8) ? a2[0][j2] : a2[1][j2];
          a3[j2] = keep + dpp<ROR8>(send);
        }
        const float s0 = dpp<0x124>(a3[1]), s1 = dpp<0x12C>(a3[0]);
        gu[m] = (c & 4) ? a3[1] + s0 : a3[0] + s1;
      }
      *reinterpret_cast<float2*>(lds + L_FP + wave * 512 + lane * 8) = make_float2(gu[0], gu[1]);
    }
    const float xn = rbf(__uint_as_float((unsigned)xe << 16) - mb) / den;
    const unsigned X = packbf(xn, xn);
    f32x4 qa = {0.f, 0.f, 0.f, 0.f};
#define QSTEP(t) qa = mfma(diag(dppu<0x150 + t>(X), mask), wqv[t], qa);
    QSTEP(0) QSTEP(1) QSTEP(2) QSTEP(3) QSTEP(4) QSTEP(5) QSTEP(6) QSTEP(7)
    QSTEP(8) QSTEP(9) QSTEP(10) QSTEP(11) QSTEP(12) QSTEP(13) QSTEP(14) QSTEP(15)
#undef QSTEP
    if (r < 2) *reinterpret_cast<f32x4*>(lds + L_QP + wave * 512 + (8 * c + 4 * r) * 4) = qa;
    __syncthreads();
    float q0 = 0.f, q1 = 0.f;
#pragma unroll
    for (int w = 0; w < NWAVE; ++w) {
      const float2 v = *reinterpret_cast<const float2*>(lds + L_QP + w * 512 + lane * 8);
      q0 += v.x; q1 += v.y;
    }
    *reinterpret_cast<unsigned*>(lds + L_QB + wave * 256 + lane * 4) = packbf(q0, q1);
    if (has_ffn && wave < 4) {
      float g = 0.f, u = 0.f;
#pragma unroll
      for (int w = 0; w < NWAVE; ++w) {
        const float2 v = *reinterpret_cast<const float2*>(lds + L_FP + w * 512 + lane * 8);
        g += v.x; u += v.y;
      }
      const float hv = rbf(gelu_tanh(rbf(g))) * rbf(u);
      *reinterpret_cast<unsigned*>(lds + L_HX + wave * 256 + (((c >> 3) * 4 + r) * 8 + (c & 3) * 2 + ((c >> 2) & 1)) * 4) = packbf(hv, hv);
    }
    __builtin_amdgcn_fence(__ATOMIC_RELEASE, "wavefront");
    __builtin_amdgcn_wave_barrier();
    __builtin_amdgcn_fence(__ATOMIC_ACQUIRE, "wavefront");
#pragma unroll
    for (int k = 0; k < 4; ++k) qf[k] = *reinterpret_cast<const u32x4*>(lds + L_QB + wave * 256 + k * 64 + r * 16);
    if (has_ffn && wave < 4) {
      const u32x4 h0 = *reinterpret_cast<const u32x4*>(lds + L_HX + wave * 256 + ((c >> 3) * 4 + r) * 32);
      const u32x4 h1 = *reinterpret_cast<const u32x4*>(lds + L_HX + wave * 256 + ((c >> 3) * 4 + r) * 32 + 16);
      f32x4 d = {0.f, 0.f, 0.f, 0.f};
#pragma unroll
      for (int t = 0; t < 4; ++t) d = mfma(diag(h0[t], mask), dv[t], d);
#pragma unroll
      for (int t = 0; t < 4; ++t) d = mfma(diag(h1[t], mask), dv[4 + t], d);
      *reinterpret_cast<f32x4*>(reinterpret_cast<float*>(ws + WS_F) + ((size_t)(r >> 1) * NUNIT + unit) * DM + wave * 128 + 8 * c + 4 * (r & 1)) = d;
    }
  }

  float m = -INFINITY, l = 0.f;
  f32x4 acc = {0.f, 0.f, 0.f, 0.f};
#define STEP(KR, VR, REFILL, off)                                                        \
  {                                                                                      \
    f32x4 S = {0.f, 0.f, 0.f, 0.f};                                                      \
    _Pragma("unroll") for (int t = 0; t < 4; ++t) S = mfma(KR[t], qf[t], S);             \
    SB                                                                                   \
    if (REFILL) { LDK(KR, off) }                                                         \
    SB                                                                                   \
    float sb[4];                                                                         \
    _Pragma("unroll") for (int k = 0; k < 4; ++k) sb[k] = rbf(rbf(S[k]) * SCALE);        \
    float mx = fmaxf(fmaxf(sb[0], sb[1]), fmaxf(sb[2], sb[3]));                          \
    mx = fmaxf(xrow_max(mx), m);                                                         \
    const float alpha = __builtin_amdgcn_exp2f((m - mx) * LOG2E);                        \
    const float nb = -mx * LOG2E;                                                        \
    m = mx; l *= alpha; acc *= alpha;                                                    \
    _Pragma("unroll") for (int k = 0; k < 4; ++k) {                                      \
      const float p = __builtin_amdgcn_exp2f(fmaf(sb[k], LOG2E, nb));                    \
      l += p;                                                                            \
      const float y = (c & 8) ? p - rbf(p) : p;                                          \
      acc = mfma(diag(packbf(y, y), mask), VR[k], acc);                                  \
    }                                                                                    \
    SB                                                                                   \
    if (REFILL) { LDV(VR, off) }                                                         \
    SB                                                                                   \
  }
  int k = 2;
#pragma unroll 1
  for (; k + 1 < n; k += 2) {
    kp += 2 * TSTR; vp += 2 * TSTR;
    STEP(Ka, Va, true, 0)
    STEP(Kb, Vb, true, TSTR)
  }
  if (k < n) {
    STEP(Ka, Va, true, 2 * TSTR)
    STEP(Kb, Vb, false, 0)
    STEP(Ka, Va, false, 0)
  } else {
    STEP(Ka, Va, false, 0)
    STEP(Kb, Vb, false, 0)
  }
#undef STEP

  {
    l = xrow_sum(l);
    f32x4 o;
#pragma unroll
    for (int k2 = 0; k2 < 4; ++k2) o[k2] = swap32_add(acc[k2], acc[k2]);
    if (r < 2) *reinterpret_cast<f32x4*>(lds + L_OW + wave * 512 + (8 * c + 4 * r) * 4) = o;
    if (lane == 0) *reinterpret_cast<float2*>(lds + L_ML + wave * 8) = make_float2(m, l);
  }
  u32x4 wf[16];
#pragma unroll
  for (int k2 = 0; k2 < 16; ++k2) wf[k2] = ldnt(wo + (size_t)(h * DH + 16 * wave + 4 * r + (k2 >> 2)) * DM + 8 * c + (k2 & 3) * 128);
  __syncthreads();
  float Mb = -INFINITY, Lb = 0.f;
  {
    float2 mlw[NWAVE];
#pragma unroll
    for (int w = 0; w < NWAVE; ++w) { mlw[w] = *reinterpret_cast<const float2*>(lds + L_ML + w * 8); Mb = fmaxf(Mb, mlw[w].x); }
    float o = 0.f;
#pragma unroll
    for (int w = 0; w < NWAVE; ++w) {
      const float cw = __builtin_amdgcn_exp2f((mlw[w].x - Mb) * LOG2E);
      Lb = fmaf(cw, mlw[w].y, Lb);
      o = fmaf(cw, *reinterpret_cast<const float*>(lds + L_OW + w * 512 + (wave * 16 + 4 * r + (c & 3)) * 4), o);
    }
    const float y = (c & 8) ? o - rbf(o) : o;
    const unsigned X = packbf(y, y);
    const u32x4 A0 = diag(dppu<0x00>(X), mask), A1 = diag(dppu<0x55>(X), mask);
    const u32x4 A2 = diag(dppu<0xAA>(X), mask), A3 = diag(dppu<0xFF>(X), mask);
#pragma unroll
    for (int sg = 0; sg < 4; ++sg) {
      f32x4 d = mfma(A0, wf[sg], f32x4{0.f, 0.f, 0.f, 0.f});
      d = mfma(A1, wf[4 + sg], d);
      d = mfma(A2, wf[8 + sg], d);
      d = mfma(A3, wf[12 + sg], d);
      *reinterpret_cast<f32x4*>(lds + L_WO + wave * 4096 + (r >> 1) * 2048 + (sg * 128 + 8 * c + 4 * (r & 1)) * 4) = d;
    }
  }
  __syncthreads();
  {
    float y = 0.f;
#pragma unroll
    for (int w = 0; w < NWAVE; ++w)
      y += *reinterpret_cast<const float*>(lds + L_WO + w * 4096 + tid * 4) + *reinterpret_cast<const float*>(lds + L_WO + w * 4096 + 2048 + tid * 4);
    const int slot = b * NSLOT + h * NSPLIT + s;
    reinterpret_cast<float*>(ws + WS_Y)[(size_t)slot * DM + tid] = y;
    if (tid == 0) *reinterpret_cast<float2*>(ws + WS_ML + (size_t)slot * 8) = make_float2(Mb * LOG2E, Lb);
  }
}

__global__ void __launch_bounds__(512)
k_final(const u16* __restrict__ x, const unsigned char* __restrict__ ws, u16* __restrict__ out) {
  __shared__ __attribute__((aligned(16))) float red[NWAVE][2][8];
  const int tid = threadIdx.x, lane = tid & 63, wave = tid >> 6;
  const int b = blockIdx.x & 1, cg = blockIdx.x >> 1;
  const int row = tid >> 1, half = tid & 1;
  f32x4 sa = {0.f, 0.f, 0.f, 0.f}, sf = {0.f, 0.f, 0.f, 0.f};
  u16 xo = 0;
  if (tid < 8) xo = x[b * DM + cg * 8 + tid];
  if (row < NUNIT) sf = *reinterpret_cast<const f32x4*>(ws + WS_F + (((size_t)b * NUNIT + row) * DM + cg * 8 + half * 4) * 4);
  if (row < NSLOT) {
    const f32x4 y = *reinterpret_cast<const f32x4*>(ws + WS_Y + (((size_t)b * NSLOT + row) * DM + cg * 8 + half * 4) * 4);
    const f32x4* mlp = reinterpret_cast<const f32x4*>(ws + WS_ML + ((size_t)b * NSLOT + (row & ~3)) * 8);
    const f32x4 a = mlp[0], c4 = mlp[1];
    const float M = fmaxf(fmaxf(a[0], a[2]), fmaxf(c4[0], c4[2]));
    const float e0 = __builtin_amdgcn_exp2f(a[0] - M), e1 = __builtin_amdgcn_exp2f(a[2] - M);
    const float e2 = __builtin_amdgcn_exp2f(c4[0] - M), e3 = __builtin_amdgcn_exp2f(c4[2] - M);
    const float L = fmaf(e0, a[1], fmaf(e1, a[3], fmaf(e2, c4[1], e3 * c4[3])));
    const int sp = row & 3;
    const float e = sp == 0 ? e0 : sp == 1 ? e1 : sp == 2 ? e2 : e3;
    sa = y * (e / L);
  }
  float v[8] = {sa[0], sa[1], sa[2], sa[3], sf[0], sf[1], sf[2], sf[3]};
#pragma unroll
  for (int k = 0; k < 8; ++k) {
    float t = v[k];
    t += dpp<XOR2>(t); t += dpp<ROR4>(t); t += dpp<ROR8>(t);
    v[k] = xrow_sum(t);
  }
  if (lane < 2) {
    *reinterpret_cast<f32x4*>(&red[wave][lane][0]) = f32x4{v[0], v[1], v[2], v[3]};
    *reinterpret_cast<f32x4*>(&red[wave][lane][4]) = f32x4{v[4], v[5], v[6], v[7]};
  }
  __syncthreads();
  if (tid < 8) {
    float s = 0.f, f = 0.f;
#pragma unroll
    for (int w = 0; w < NWAVE; ++w) { s += red[w][tid >> 2][tid & 3]; f += red[w][tid >> 2][4 + (tid & 3)]; }
    s = rbf(s); f = rbf(f);
    const float g = rbf(f / (1.f + __expf(-f)));
    const float o = __uint_as_float((unsigned)xo << 16) + rbf(s * g);
    out[b * DM + cg * 8 + tid] = __builtin_bit_cast(u16, (__bf16)o);
  }
}

extern "C" void kernel_launch(void* const* d_in, const int* in_sizes, int n_in, void* d_out, int out_size, void* d_ws,
                              size_t ws_size, hipStream_t stream) {
  const u16* x = (const u16*)d_in[0];
  const u16* kc = (const u16*)d_in[1];
  const u16* vc = (const u16*)d_in[2];
  const u16* wq = (const u16*)d_in[3];
  const u16* wo = (const u16*)d_in[4];
  const u16* wg = (const u16*)d_in[6];
  const u16* wu = (const u16*)d_in[7];
  const u16* wd = (const u16*)d_in[8];
  k_main<<<256, 512, 0, stream>>>(x, kc, vc, wq, wo, wg, wu, wd, (unsigned char*)d_ws);
  k_final<<<128, 512, 0, stream>>>(x, (const unsigned char*)d_ws, (u16*)d_out);
}
```

```cpp
#include <hip/hip_runtime.h>
#include <hip/hip_bf16.h>
#include <stdint.h>

using u32x4 = __attribute__((ext_vector_type(4))) unsigned;
using f32x4 = __attribute__((ext_vector_type(4))) float;
using bf16x2 = __attribute__((ext_vector_type(2))) __bf16;
using bf16x8 = __attribute__((ext_vector_type(8))) __bf16;
typedef unsigned short u16;

constexpr int DM = 512, HQ = 32, SK = 8192, DH = 128, FF = 1376;
constexpr int NSPLIT = 4;
constexpr int NWAVE = 8;
constexpr int NTILE = SK / 16;
constexpr int NT_WAVE = NTILE / NSPLIT / NWAVE;
constexpr int NUNIT = FF / 32;
constexpr int NSLOT = HQ * NSPLIT;
constexpr float LOG2E = 1.4426950408889634f;
constexpr float SCALE = 0.08838834764831845f;

constexpr size_t WS_Y = 0;
constexpr size_t WS_ML = WS_Y + (size_t)2 * NSLOT * DM * 4;
constexpr int NFP = 2 * NUNIT;
constexpr size_t WS_F = WS_ML + (size_t)2 * NSLOT * 2 * 4;

__device__ __forceinline__ float dot2(unsigned a, unsigned b, float acc) {
  return __builtin_amdgcn_fdot2_f32_bf16(__builtin_bit_cast(bf16x2, a), __builtin_bit_cast(bf16x2, b), acc, false);
}
template <int CTRL> __device__ __forceinline__ float dpp(float x) {
  return __builtin_bit_cast(float, __builtin_amdgcn_mov_dpp(__builtin_bit_cast(int, x), CTRL, 0xf, 0xf, true));
}
template <int CTRL> __device__ __forceinline__ unsigned dppu(unsigned x) {
  return (unsigned)__builtin_amdgcn_mov_dpp((int)x, CTRL, 0xf, 0xf, true);
}
constexpr int ROR8 = 0x128, ROR4 = 0x124, ROR2 = 0x122, ROR1 = 0x121, XOR1 = 0xB1, XOR2 = 0x4E, HMIR = 0x141;
__device__ __forceinline__ float row16_sum(float x) {
  x += dpp<ROR8>(x); x += dpp<ROR4>(x); x += dpp<ROR2>(x); x += dpp<ROR1>(x); return x;
}
__device__ __forceinline__ float swap32_add(float a, float b) {
  auto t = __builtin_amdgcn_permlane32_swap(__float_as_uint(a), __float_as_uint(b), false, false);
  return __uint_as_float(t[0]) + __uint_as_float(t[1]);
}
__device__ __forceinline__ float swap16_add(float a, float b) {
  auto t = __builtin_amdgcn_permlane16_swap(__float_as_uint(a), __float_as_uint(b), false, false);
  return __uint_as_float(t[0]) + __uint_as_float(t[1]);
}
__device__ __forceinline__ float xrow_sum(float x) { x = swap16_add(x, x); return swap32_add(x, x); }
__device__ __forceinline__ float xrow_max(float x) {
  auto s = __builtin_amdgcn_permlane16_swap(__float_as_uint(x), __float_as_uint(x), false, false);
  x = fmaxf(__uint_as_float(s[0]), __uint_as_float(s[1]));
  auto t = __builtin_amdgcn_permlane32_swap(__float_as_uint(x), __float_as_uint(x), false, false);
  return fmaxf(__uint_as_float(t[0]), __uint_as_float(t[1]));
}
__device__ __forceinline__ float wave_sum(float x) { return xrow_sum(row16_sum(x)); }
__device__ __forceinline__ float rbf(float x) { return (float)(__bf16)x; }
__device__ __forceinline__ float bflo(unsigned v) { return __uint_as_float(v << 16); }
__device__ __forceinline__ float bfhi(unsigned v) { return __uint_as_float(v & 0xffff0000u); }
__device__ __forceinline__ unsigned packbf(float lo, float hi) {
  bf16x2 r = {(__bf16)lo, (__bf16)hi};
  return __builtin_bit_cast(unsigned, r);
}
__device__ __forceinline__ u32x4 ldnt(const void* p) { return __builtin_nontemporal_load(reinterpret_cast<const u32x4*>(p)); }
__device__ __forceinline__ u32x4 ld16(const void* p) { return *reinterpret_cast<const u32x4*>(p); }
__device__ __forceinline__ f32x4 mfma(u32x4 a, u32x4 b, f32x4 c) {
  return __builtin_amdgcn_mfma_f32_16x16x32_bf16(__builtin_bit_cast(bf16x8, a), __builtin_bit_cast(bf16x8, b), c, 0, 0, 0);
}
__device__ __forceinline__ u32x4 diag(unsigned x, u32x4 mask) { return u32x4{x & mask[0], x & mask[1], x & mask[2], x & mask[3]}; }

__device__ __forceinline__ void ln_stats(u32x4 xv, float& mb, float& den) {
  float xf[8];
#pragma unroll
  for (int k = 0; k < 4; ++k) { xf[2 * k] = bflo(xv[k]); xf[2 * k + 1] = bfhi(xv[k]); }
  const float s = ((xf[0] + xf[1]) + (xf[2] + xf[3])) + ((xf[4] + xf[5]) + (xf[6] + xf[7]));
  const float mean = wave_sum(s) * (1.f / DM);
  float q0 = 0.f, q1 = 0.f;
#pragma unroll
  for (int k = 0; k < 4; ++k) {
    const float d0 = xf[2 * k] - mean, d1 = xf[2 * k + 1] - mean;
    q0 = fmaf(d0, d0, q0); q1 = fmaf(d1, d1, q1);
  }
  const float var = wave_sum(q0 + q1) * (1.f / DM);
  mb = rbf(mean);
  den = rbf(sqrtf(rbf(rbf(var) + 1e-5f)));
}
__device__ __forceinline__ float gelu_tanh(float x) {
  const float u2 = 2.f * 0.7978845608028654f * (x + 0.044715f * x * x * x);
  const float th = 1.f - 2.f / (1.f + __expf(u2));
  return 0.5f * x * (1.f + th);
}

constexpr int L_WO = 0;
constexpr int L_QP = L_WO + NWAVE * 4096;
constexpr int L_QB = L_QP + NWAVE * 512;
constexpr int L_OW = L_QB + NWAVE * 256;
constexpr int L_ML = L_OW + NWAVE * 512;
constexpr int L_FP = L_ML + NWAVE * 8;
constexpr int L_HX = L_FP + NWAVE * 512;
constexpr int L_END = L_HX + NWAVE * 256;

__global__ void __launch_bounds__(512)
k_main(const u16* __restrict__ x, const u16* __restrict__ kc, const u16* __restrict__ vc, const u16* __restrict__ wq,
       const u16* __restrict__ wo, const u16* __restrict__ wg, const u16* __restrict__ wu, const u16* __restrict__ wd,
       unsigned char* __restrict__ ws) {
  __shared__ __attribute__((aligned(16))) unsigned char lds[L_END];
  const int tid = threadIdx.x, lane = tid & 63, wave = __builtin_amdgcn_readfirstlane(tid >> 6);
  const int r = lane >> 4, c = lane & 15;
  const int bid = blockIdx.x, g8 = bid & 7, i = bid >> 3;
  const int h = g8 * 4 + (i >> 3), b = (i >> 2) & 1, s = i & 3;
  const int unit = g8 + 8 * (i >> 2);
  const bool has_ffn = unit < NUNIT && s == 3;
  const int t0 = s * (NTILE / NSPLIT);

  u32x4 mask;
#pragma unroll
  for (int k = 0; k < 4; ++k) mask[k] = ((c & 7) >> 1) == k ? ((c & 1) ? 0xffff0000u : 0x0000ffffu) : 0u;

  const u32x4 xv = ld16(x + b * DM + 8 * lane);
  const u16 xe = x[b * DM + wave * 64 + lane];
  __builtin_amdgcn_sched_barrier(0);
  u32x4 Ka[4], Va[4], Kb[4], Vb[4];
  const size_t kvoff = ((size_t)(b * HQ + h) * SK + (size_t)(t0 + wave) * 16) * DH;
  const unsigned char* kp = reinterpret_cast<const unsigned char*>(kc + kvoff) + (has_ffn ? r * 1024 + c * 16 : c * 256 + r * 16);
  const unsigned char* kq = reinterpret_cast<const unsigned char*>(kc + kvoff) + r * 1024 + c * 16;
  const unsigned char* vp = reinterpret_cast<const unsigned char*>(vc + kvoff) + r * 1024 + c * 16;
  constexpr int TSTR = NWAVE * 4096;
#define LDK(KR, off, KS) _Pragma("unroll") for (int t = 0; t < 4; ++t) KR[t] = ldnt(((off) ? kq : kp) + (off) + t * KS);
#define LDV(VR, off) _Pragma("unroll") for (int t = 0; t < 4; ++t) VR[t] = ldnt(vp + (off) + t * 256);
#define SB __builtin_amdgcn_sched_barrier(0);
  u32x4 qf[4], qr, dv[4];
  {
    u32x4 xo, gv[4], uv[4];
    uint2 xr0, xr1;
    SB
    u32x4 wqv[16];
    const u16* p = wq + (size_t)(wave * 64 + 16 * r) * (HQ * DH) + h * DH + 8 * c;
#pragma unroll
    for (int t = 0; t < 16; ++t) wqv[t] = ldnt(p + (size_t)t * (HQ * DH));
    SB
    if (has_ffn) {
      xo = ld16(x + (1 - b) * DM + 8 * lane);
      xr0 = *reinterpret_cast<const uint2*>(x + wave * 64 + 4 * (lane >> 2));
      xr1 = *reinterpret_cast<const uint2*>(x + DM + wave * 64 + 4 * (lane >> 2));
      const size_t fo = (size_t)(wave * 64 + 4 * (lane >> 2)) * FF + unit * 32 + 8 * (lane & 3);
#pragma unroll
      for (int t = 0; t < 4; ++t) gv[t] = ldnt(wg + fo + (size_t)t * FF);
#pragma unroll
      for (int t = 0; t < 4; ++t) uv[t] = ldnt(wu + fo + (size_t)t * FF);
    }
    SB
    const int ks = has_ffn ? 256 : 64;
    LDK(Ka, 0, ks) SB LDV(Va, 0) SB LDK(Kb, TSTR, 256) SB LDV(Vb, TSTR) SB
    float mb, den;
    ln_stats(xv, mb, den);
    const float xn = rbf(__uint_as_float((unsigned)xe << 16) - mb) / den;
    const unsigned X = packbf(xn, xn);
    f32x4 qa = {0.f, 0.f, 0.f, 0.f};
#define QSTEP(t) qa = mfma(diag(dppu<0x150 + t>(X), mask), wqv[t], qa);
    QSTEP(0) QSTEP(1) QSTEP(2) QSTEP(3) QSTEP(4) QSTEP(5) QSTEP(6) QSTEP(7)
    QSTEP(8) QSTEP(9) QSTEP(10) QSTEP(11) QSTEP(12) QSTEP(13) QSTEP(14) QSTEP(15)
#undef QSTEP
    if (r < 2) *reinterpret_cast<f32x4*>(lds + L_QP + wave * 512 + (8 * c + 4 * r) * 4) = qa;
    __syncthreads();
    float q0 = 0.f, q1 = 0.f;
#pragma unroll
    for (int w = 0; w < NWAVE; ++w) {
      const float2 v = *reinterpret_cast<const float2*>(lds + L_QP + w * 512 + lane * 8);
      q0 += v.x; q1 += v.y;
    }
    *reinterpret_cast<unsigned*>(lds + L_QB + wave * 256 + lane * 4) = packbf(q0, q1);
    __builtin_amdgcn_fence(__ATOMIC_RELEASE, "wavefront");
    __builtin_amdgcn_wave_barrier();
    __builtin_amdgcn_fence(__ATOMIC_ACQUIRE, "wavefront");
#pragma unroll
    for (int k = 0; k < 4; ++k) qf[k] = *reinterpret_cast<const u32x4*>(lds + L_QB + wave * 256 + k * 64 + r * 16);
    qr = *reinterpret_cast<const u32x4*>(lds + L_QB + wave * 256 + c * 16);
    if (has_ffn) {
      float mo, deo;
      ln_stats(xo, mo, deo);
      const float m0 = b ? mo : mb, d0 = b ? deo : den, m1 = b ? mb : mo, d1 = b ? den : deo;
      unsigned xp0[2], xp1[2];
      xp0[0] = packbf(rbf(bflo(xr0.x) - m0) / d0, rbf(bfhi(xr0.x) - m0) / d0);
      xp0[1] = packbf(rbf(bflo(xr0.y) - m0) / d0, rbf(bfhi(xr0.y) - m0) / d0);
      xp1[0] = packbf(rbf(bflo(xr1.x) - m1) / d1, rbf(bfhi(xr1.x) - m1) / d1);
      xp1[1] = packbf(rbf(bflo(xr1.y) - m1) / d1, rbf(bfhi(xr1.y) - m1) / d1);
      float f[2][2][8];
#pragma unroll
      for (int k = 0; k < 32; ++k) (&f[0][0][0])[k] = 0.f;
#pragma unroll
      for (int m = 0; m < 2; ++m)
#pragma unroll
        for (int k = 0; k < 4; ++k) {
          const unsigned glo = __builtin_amdgcn_perm(gv[2 * m + 1][k], gv[2 * m][k], 0x05040100u);
          const unsigned ghi = __builtin_amdgcn_perm(gv[2 * m + 1][k], gv[2 * m][k], 0x07060302u);
          const unsigned ulo = __builtin_amdgcn_perm(uv[2 * m + 1][k], uv[2 * m][k], 0x05040100u);
          const unsigned uhi = __builtin_amdgcn_perm(uv[2 * m + 1][k], uv[2 * m][k], 0x07060302u);
          f[0][0][2 * k] = dot2(glo, xp0[m], f[0][0][2 * k]); f[0][0][2 * k + 1] = dot2(ghi, xp0[m], f[0][0][2 * k + 1]);
          f[0][1][2 * k] = dot2(glo, xp1[m], f[0][1][2 * k]); f[0][1][2 * k + 1] = dot2(ghi, xp1[m], f[0][1][2 * k + 1]);
          f[1][0][2 * k] = dot2(ulo, xp0[m], f[1][0][2 * k]); f[1][0][2 * k + 1] = dot2(uhi, xp0[m], f[1][0][2 * k + 1]);
          f[1][1][2 * k] = dot2(ulo, xp1[m], f[1][1][2 * k]); f[1][1][2 * k + 1] = dot2(uhi, xp1[m], f[1][1][2 * k + 1]);
        }
      float gu[2];
#pragma unroll
      for (int m = 0; m < 2; ++m) {
        float a2[2][2];
#pragma unroll
        for (int bb = 0; bb < 2; ++bb)
#pragma unroll
          for (int j2 = 0; j2 < 2; ++j2) {
            const float e0 = swap32_add(f[m][bb][4 * j2 + 0], f[m][bb][4 * j2 + 2]);
            const float e1 = swap32_add(f[m][bb][4 * j2 + 1], f[m][bb][4 * j2 + 3]);
            a2[bb][j2] = swap16_add(e0, e1);
          }
        float a3[2];
#pragma unroll
        for (int j2 = 0; j2 < 2; ++j2) {
          const float keep = (c & 8) ? a2[1][j2] : a2[0][j2], send = (c & 8) ? a2[0][j2] : a2[1][j2];
          a3[j2] = keep + dpp<ROR8>(send);
        }
        const float s0 = dpp<0x124>(a3[1]), s1 = dpp<0x12C>(a3[0]);
        gu[m] = (c & 4) ? a3[1] + s0 : a3[0] + s1;
      }
      *reinterpret_cast<float2*>(lds + L_FP + wave * 512 + lane * 8) = make_float2(gu[0], gu[1]);
      SB
#pragma unroll
      for (int t = 0; t < 4; ++t) dv[t] = ldnt(wd + (size_t)(unit * 32 + (wave >> 2) * 16 + 4 * t + r) * DM + (wave & 3) * 128 + 8 * c);
      SB
    }
  }

  float m = -INFINITY, l = 0.f;
  f32x4 acc = {0.f, 0.f, 0.f, 0.f};
#define STEP(KR, VR, REFILL, off, KS)                                                      \
  {                                                                                      \
    f32x4 S = {0.f, 0.f, 0.f, 0.f};                                                      \
    if (KS == 64) {                                                                      \
      _Pragma("unroll") for (int t = 0; t < 4; ++t) S = mfma(KR[t], qf[t], S);           \
    } else {                                                                             \
      _Pragma("unroll") for (int j = 0; j < 4; ++j)                                      \
        _Pragma("unroll") for (int t = 0; t < 4; ++t) S[t] = dot2(KR[t][j], qr[j], S[t]); \
      _Pragma("unroll") for (int t = 0; t < 4; ++t) S[t] += dpp<ROR8>(S[t]);             \
      _Pragma("unroll") for (int t = 0; t < 4; ++t) S[t] += dpp<ROR4>(S[t]);             \
      _Pragma("unroll") for (int t = 0; t < 4; ++t) S[t] += dpp<ROR2>(S[t]);             \
      _Pragma("unroll") for (int t = 0; t < 4; ++t) S[t] += dpp<ROR1>(S[t]);             \
    }                                                                                    \
    SB                                                                                   \
    if (REFILL) { LDK(KR, off, KS) }                                                     \
    SB                                                                                   \
    float sb[4];                                                                         \
    _Pragma("unroll") for (int k = 0; k < 4; ++k) sb[k] = rbf(rbf(S[k]) * SCALE);        \
    float mx = fmaxf(fmaxf(sb[0], sb[1]), fmaxf(sb[2], sb[3]));                          \
    mx = fmaxf(xrow_max(mx), m);                                                         \
    const float alpha = __builtin_amdgcn_exp2f((m - mx) * LOG2E);                        \
    const float nb = -mx * LOG2E;                                                        \
    m = mx; l *= alpha; acc *= alpha;                                                    \
    _Pragma("unroll") for (int k = 0; k < 4; ++k) {                                      \
      const float p = __builtin_amdgcn_exp2f(fmaf(sb[k], LOG2E, nb));                    \
      l += p;                                                                            \
      const float y = (c & 8) ? p - rbf(p) : p;                                          \
      acc = mfma(diag(packbf(y, y), mask), VR[k], acc);                                  \
    }                                                                                    \
    SB                                                                                   \
    if (REFILL) { LDV(VR, off) }                                                         \
    SB                                                                                   \
  }
#define MAINLOOP(KS)                                                                     \
  {                                                                                      \
    _Pragma("unroll 1") for (int k = 2; k < NT_WAVE; k += 2) {                           \
      kp += 2 * TSTR; kq += 2 * TSTR; vp += 2 * TSTR;                                    \
      STEP(Ka, Va, true, 0, KS)                                                          \
      STEP(Kb, Vb, true, TSTR, 256)                                                      \
    }                                                                                    \
    STEP(Ka, Va, false, 0, KS)                                                           \
    STEP(Kb, Vb, false, 0, 256)                                                          \
  }
  if (has_ffn) MAINLOOP(256) else MAINLOOP(64)
#undef MAINLOOP
#undef STEP

  {
    l = xrow_sum(l);
    f32x4 o;
#pragma unroll
    for (int k2 = 0; k2 < 4; ++k2) o[k2] = swap32_add(acc[k2], acc[k2]);
    if (r < 2) *reinterpret_cast<f32x4*>(lds + L_OW + wave * 512 + (8 * c + 4 * r) * 4) = o;
    if (lane == 0) *reinterpret_cast<float2*>(lds + L_ML + wave * 8) = make_float2(m, l);
  }
  u32x4 wf[16];
#pragma unroll
  for (int k2 = 0; k2 < 16; ++k2) wf[k2] = ldnt(wo + (size_t)(h * DH + 16 * wave + 4 * r + (k2 >> 2)) * DM + 8 * c + (k2 & 3) * 128);
  __syncthreads();
    if (has_ffn) {
      float g = 0.f, u = 0.f;
#pragma unroll
      for (int w = 0; w < NWAVE; ++w) {
        const float2 v = *reinterpret_cast<const float2*>(lds + L_FP + w * 512 + lane * 8);
        g += v.x; u += v.y;
      }
      const float hv = rbf(gelu_tanh(rbf(g))) * rbf(u);
      *reinterpret_cast<unsigned*>(lds + L_HX + wave * 256 + (((c >> 3) * 4 + r) * 8 + (c & 3) * 2 + ((c >> 2) & 1)) * 4) = packbf(hv, hv);
    }
    __builtin_amdgcn_fence(__ATOMIC_RELEASE, "wavefront");
    __builtin_amdgcn_wave_barrier();
    __builtin_amdgcn_fence(__ATOMIC_ACQUIRE, "wavefront");
    if (has_ffn) {
      const u32x4 h0 = *reinterpret_cast<const u32x4*>(lds + L_HX + wave * 256 + ((c >> 3) * 4 + r) * 32 + (wave >> 2) * 16);
      f32x4 d = {0.f, 0.f, 0.f, 0.f};
#pragma unroll
      for (int t = 0; t < 4; ++t) d = mfma(diag(h0[t], mask), dv[t], d);
      *reinterpret_cast<f32x4*>(reinterpret_cast<float*>(ws + WS_F) + ((size_t)(r >> 1) * NFP + unit * 2 + (wave >> 2)) * DM + (wave & 3) * 128 + 8 * c + 4 * (r & 1)) = d;
    }
  float Mb = -INFINITY, Lb = 0.f;
  {
    float2 mlw[NWAVE];
#pragma unroll
    for (int w = 0; w < NWAVE; ++w) { mlw[w] = *reinterpret_cast<const float2*>(lds + L_ML + w * 8); Mb = fmaxf(Mb, mlw[w].x); }
    float o = 0.f;
#pragma unroll
    for (int w = 0; w < NWAVE; ++w) {
      const float cw = __builtin_amdgcn_exp2f((mlw[w].x - Mb) * LOG2E);
      Lb = fmaf(cw, mlw[w].y, Lb);
      o = fmaf(cw, *reinterpret_cast<const float*>(lds + L_OW + w * 512 + (wave * 16 + 4 * r + (c & 3)) * 4), o);
    }
    const float y = (c & 8) ? o - rbf(o) : o;
    const unsigned X = packbf(y, y);
    const u32x4 A0 = diag(dppu<0x00>(X), mask), A1 = diag(dppu<0x55>(X), mask);
    const u32x4 A2 = diag(dppu<0xAA>(X), mask), A3 = diag(dppu<0xFF>(X), mask);
#pragma unroll
    for (int sg = 0; sg < 4; ++sg) {
      f32x4 d = mfma(A0, wf[sg], f32x4{0.f, 0.f, 0.f, 0.f});
      d = mfma(A1, wf[4 + sg], d);
      d = mfma(A2, wf[8 + sg], d);
      d = mfma(A3, wf[12 + sg], d);
      *reinterpret_cast<f32x4*>(lds + L_WO + wave * 4096 + (r >> 1) * 2048 + (sg * 128 + 8 * c + 4 * (r & 1)) * 4) = d;
    }
  }
  __syncthreads();
  {
    float y = 0.f;
#pragma unroll
    for (int w = 0; w < NWAVE; ++w)
      y += *reinterpret_cast<const float*>(lds + L_WO + w * 4096 + tid * 4) + *reinterpret_cast<const float*>(lds + L_WO + w * 4096 + 2048 + tid * 4);
    const int slot = b * NSLOT + h * NSPLIT + s;
    reinterpret_cast<float*>(ws + WS_Y)[(size_t)slot * DM + tid] = y;
    if (tid == 0) *reinterpret_cast<float2*>(ws + WS_ML + (size_t)slot * 8) = make_float2(Mb * LOG2E, Lb);
  }
}

__global__ void __launch_bounds__(256)
k_final(const u16* __restrict__ x, const unsigned char* __restrict__ ws, u16* __restrict__ out) {
  __shared__ __attribute__((aligned(16))) float red[4][2][8];
  const int tid = threadIdx.x, lane = tid & 63, wave = tid >> 6;
  const int b = blockIdx.x & 1, cg = blockIdx.x >> 1;
  const int row = tid >> 1, half = tid & 1;
  const f32x4 y = *reinterpret_cast<const f32x4*>(ws + WS_Y + (((size_t)b * NSLOT + row) * DM + cg * 8 + half * 4) * 4);
  const f32x4* mlp = reinterpret_cast<const f32x4*>(ws + WS_ML + ((size_t)b * NSLOT + (row & ~3)) * 8);
  const f32x4 a = mlp[0], c4 = mlp[1];
  const int frow = row < NFP ? row : 0;
  f32x4 sf = *reinterpret_cast<const f32x4*>(ws + WS_F + (((size_t)b * NFP + frow) * DM + cg * 8 + half * 4) * 4);
  const u16 xo = x[b * DM + cg * 8 + (tid & 7)];
  __builtin_amdgcn_sched_barrier(0);
  if (row >= NFP) sf = f32x4{0.f, 0.f, 0.f, 0.f};
  const float M = fmaxf(fmaxf(a[0], a[2]), fmaxf(c4[0], c4[2]));
  const float e0 = __builtin_amdgcn_exp2f(a[0] - M), e1 = __builtin_amdgcn_exp2f(a[2] - M);
  const float e2 = __builtin_amdgcn_exp2f(c4[0] - M), e3 = __builtin_amdgcn_exp2f(c4[2] - M);
  const float L = fmaf(e0, a[1], fmaf(e1, a[3], fmaf(e2, c4[1], e3 * c4[3])));
  const int sp = row & 3;
  const float e = sp == 0 ? e0 : sp == 1 ? e1 : sp == 2 ? e2 : e3;
  const f32x4 sa = y * (e / L);
  float v[8] = {sa[0], sa[1], sa[2], sa[3], sf[0], sf[1], sf[2], sf[3]};
  float u[4], w2[2];
#pragma unroll
  for (int k = 0; k < 4; ++k) u[k] = swap32_add(v[k], v[4 + k]);
#pragma unroll
  for (int k = 0; k < 2; ++k) w2[k] = swap16_add(u[k], u[2 + k]);
#pragma unroll
  for (int k = 0; k < 2; ++k) { float t = w2[k]; t += dpp<XOR2>(t); t += dpp<ROR4>(t); t += dpp<ROR8>(t); w2[k] = t; }
  if ((lane & 14) == 0)
    *reinterpret_cast<float2*>(&red[wave][lane >> 5][(lane & 1) * 4 + ((lane >> 4) & 1) * 2]) = make_float2(w2[0], w2[1]);
  __syncthreads();
  if (tid < 8) {
    float s = (red[0][0][tid] + red[1][0][tid]) + (red[2][0][tid] + red[3][0][tid]);
    float f = (red[0][1][tid] + red[1][1][tid]) + (red[2][1][tid] + red[3][1][tid]);
    s = rbf(s); f = rbf(f);
    const float g = rbf(f / (1.f + __expf(-f)));
    const float o = __uint_as_float((unsigned)xo << 16) + rbf(s * g);
    out[b * DM + cg * 8 + tid] = __builtin_bit_cast(u16, (__bf16)o);
  }
}

extern "C" void kernel_launch(void* const* d_in, const int* in_sizes, int n_in, void* d_out, int out_size, void* d_ws,
                              size_t ws_size, hipStream_t stream) {
  const u16* x = (const u16*)d_in[0];
  const u16* kc = (const u16*)d_in[1];
  const u16* vc = (const u16*)d_in[2];
  const u16* wq = (const u16*)d_in[3];
  const u16* wo = (const u16*)d_in[4];
  const u16* wg = (const u16*)d_in[6];
  const u16* wu = (const u16*)d_in[7];
  const u16* wd = (const u16*)d_in[8];
  k_main<<<256, 512, 0, stream>>>(x, kc, vc, wq, wo, wg, wu, wd, (unsigned char*)d_ws);
  k_final<<<128, 256, 0, stream>>>(x, (const unsigned char*)d_ws, (u16*)d_out);
}
```

```cpp
#include <hip/hip_runtime.h>
#include <hip/hip_bf16.h>
#include <stdint.h>

using u32x4 = __attribute__((ext_vector_type(4))) unsigned;
using f32x4 = __attribute__((ext_vector_type(4))) float;
using bf16x2 = __attribute__((ext_vector_type(2))) __bf16;
using bf16x8 = __attribute__((ext_vector_type(8))) __bf16;
typedef unsigned short u16;

constexpr int DM = 512, HQ = 32, SK = 8192, DH = 128, FF = 1376;
constexpr int NSPLIT = 4;
constexpr int NWAVE = 8;
constexpr int NTILE = SK / 16;
constexpr int NT_WAVE = NTILE / NSPLIT / NWAVE;
constexpr int NUNIT = FF / 32;
constexpr int NSLOT = HQ * NSPLIT;
constexpr float LOG2E = 1.4426950408889634f;
constexpr float SCALE = 0.08838834764831845f;

constexpr size_t WS_Y = 0;
constexpr size_t WS_ML = WS_Y + (size_t)2 * NSLOT * DM * 4;
constexpr int NFP = 2 * NUNIT;
constexpr size_t WS_F = WS_ML + (size_t)2 * NSLOT * 2 * 4;

__device__ __forceinline__ float dot2(unsigned a, unsigned b, float acc) {
  return __builtin_amdgcn_fdot2_f32_bf16(__builtin_bit_cast(bf16x2, a), __builtin_bit_cast(bf16x2, b), acc, false);
}
template <int CTRL> __device__ __forceinline__ float dpp(float x) {
  return __builtin_bit_cast(float, __builtin_amdgcn_mov_dpp(__builtin_bit_cast(int, x), CTRL, 0xf, 0xf, true));
}
template <int CTRL> __device__ __forceinline__ unsigned dppu(unsigned x) {
  return (unsigned)__builtin_amdgcn_mov_dpp((int)x, CTRL, 0xf, 0xf, true);
}
constexpr int ROR8 = 0x128, ROR4 = 0x124, ROR2 = 0x122, ROR1 = 0x121, XOR1 = 0xB1, XOR2 = 0x4E, HMIR = 0x141;
__device__ __forceinline__ float row16_sum(float x) {
  x += dpp<ROR8>(x); x += dpp<ROR4>(x); x += dpp<ROR2>(x); x += dpp<ROR1>(x); return x;
}
__device__ __forceinline__ float swap32_add(float a, float b) {
  auto t = __builtin_amdgcn_permlane32_swap(__float_as_uint(a), __float_as_uint(b), false, false);
  return __uint_as_float(t[0]) + __uint_as_float(t[1]);
}
__device__ __forceinline__ float swap16_add(float a, float b) {
  auto t = __builtin_amdgcn_permlane16_swap(__float_as_uint(a), __float_as_uint(b), false, false);
  return __uint_as_float(t[0]) + __uint_as_float(t[1]);
}
__device__ __forceinline__ float xrow_sum(float x) { x = swap16_add(x, x); return swap32_add(x, x); }
__device__ __forceinline__ float xrow_max(float x) {
  auto s = __builtin_amdgcn_permlane16_swap(__float_as_uint(x), __float_as_uint(x), false, false);
  x = fmaxf(__uint_as_float(s[0]), __uint_as_float(s[1]));
  auto t = __builtin_amdgcn_permlane32_swap(__float_as_uint(x), __float_as_uint(x), false, false);
  return fmaxf(__uint_as_float(t[0]), __uint_as_float(t[1]));
}
__device__ __forceinline__ float wave_sum(float x) { return xrow_sum(row16_sum(x)); }
__device__ __forceinline__ float rbf(float x) { return (float)(__bf16)x; }
__device__ __forceinline__ float bflo(unsigned v) { return __uint_as_float(v << 16); }
__device__ __forceinline__ float bfhi(unsigned v) { return __uint_as_float(v & 0xffff0000u); }
__device__ __forceinline__ unsigned packbf(float lo, float hi) {
  bf16x2 r = {(__bf16)lo, (__bf16)hi};
  return __builtin_bit_cast(unsigned, r);
}
__device__ __forceinline__ u32x4 ldnt(const void* p) { return __builtin_nontemporal_load(reinterpret_cast<const u32x4*>(p)); }
__device__ __forceinline__ u32x4 ld16(const void* p) { return *reinterpret_cast<const u32x4*>(p); }
__device__ __forceinline__ f32x4 mfma(u32x4 a, u32x4 b, f32x4 c) {
  return __builtin_amdgcn_mfma_f32_16x16x32_bf16(__builtin_bit_cast(bf16x8, a), __builtin_bit_cast(bf16x8, b), c, 0, 0, 0);
}
__device__ __forceinline__ u32x4 diag(unsigned x, u32x4 mask) { return u32x4{x & mask[0], x & mask[1], x & mask[2], x & mask[3]}; }

__device__ __forceinline__ void ln_stats(u32x4 xv, float& mb, float& den) {
  float xf[8];
#pragma unroll
  for (int k = 0; k < 4; ++k) { xf[2 * k] = bflo(xv[k]); xf[2 * k + 1] = bfhi(xv[k]); }
  const float s = ((xf[0] + xf[1]) + (xf[2] + xf[3])) + ((xf[4] + xf[5]) + (xf[6] + xf[7]));
  const float mean = wave_sum(s) * (1.f / DM);
  float q0 = 0.f, q1 = 0.f;
#pragma unroll
  for (int k = 0; k < 4; ++k) {
    const float d0 = xf[2 * k] - mean, d1 = xf[2 * k + 1] - mean;
    q0 = fmaf(d0, d0, q0); q1 = fmaf(d1, d1, q1);
  }
  const float var = wave_sum(q0 + q1) * (1.f / DM);
  mb = rbf(mean);
  den = rbf(sqrtf(rbf(rbf(var) + 1e-5f)));
}
__device__ __forceinline__ float gelu_tanh(float x) {
  const float u2 = 2.f * 0.7978845608028654f * (x + 0.044715f * x * x * x);
  const float th = 1.f - 2.f / (1.f + __expf(u2));
  return 0.5f * x * (1.f + th);
}

constexpr int L_WO = 0;
constexpr int L_QP = L_WO + NWAVE * 4096;
constexpr int L_QB = L_QP + NWAVE * 512;
constexpr int L_OW = L_QB + NWAVE * 256;
constexpr int L_ML = L_OW + NWAVE * 512;
constexpr int L_FP = L_ML + NWAVE * 8;
constexpr int L_HX = L_FP + NWAVE * 512;
constexpr int L_END = L_HX + NWAVE * 256;

__global__ void __launch_bounds__(512)
k_main(const u16* __restrict__ x, const u16* __restrict__ kc, const u16* __restrict__ vc, const u16* __restrict__ wq,
       const u16* __restrict__ wo, const u16* __restrict__ wg, const u16* __restrict__ wu, const u16* __restrict__ wd,
       unsigned char* __restrict__ ws) {
  __shared__ __attribute__((aligned(16))) unsigned char lds[L_END];
  const int tid = threadIdx.x, lane = tid & 63, wave = __builtin_amdgcn_readfirstlane(tid >> 6);
  const int r = lane >> 4, c = lane & 15;
  const int bid = blockIdx.x, g8 = bid & 7, i = bid >> 3;
  const int h = g8 * 4 + (i >> 3), b = (i >> 2) & 1, s = i & 3;
  const int unit = g8 + 8 * (i >> 2);
  const bool has_ffn = unit < NUNIT && s == 3;
  const int t0 = s * (NTILE / NSPLIT);

  u32x4 mask;
#pragma unroll
  for (int k = 0; k < 4; ++k) mask[k] = ((c & 7) >> 1) == k ? ((c & 1) ? 0xffff0000u : 0x0000ffffu) : 0u;

  const u32x4 xv = ld16(x + b * DM + 8 * lane);
  const u16 xe = x[b * DM + wave * 64 + lane];
  __builtin_amdgcn_sched_barrier(0);
  u32x4 Ka[4], Va[4], Kb[4], Vb[4];
  const size_t kvoff = ((size_t)(b * HQ + h) * SK + (size_t)t0 * 16) * DH;
  const auto rk = __builtin_amdgcn_make_buffer_rsrc((void*)(kc + kvoff), 0, NTILE / NSPLIT * 4096, 0x00020000);
  const auto rv = __builtin_amdgcn_make_buffer_rsrc((void*)(vc + kvoff), 0, NTILE / NSPLIT * 4096, 0x00020000);
  const int vrow = r * 1024 + c * 16;
  const int vka = has_ffn ? vrow : c * 256 + r * 16;
  int so = wave * 4096;
  constexpr int TSTR = NWAVE * 4096;
  constexpr int AUXKV = 2;
#define LDK(KR, off, KS) _Pragma("unroll") for (int t = 0; t < 4; ++t) KR[t] = __builtin_amdgcn_raw_buffer_load_b128(rk, ((off) ? vrow : vka) + t * KS, so + (off), AUXKV);
#define LDV(VR, off) _Pragma("unroll") for (int t = 0; t < 4; ++t) VR[t] = __builtin_amdgcn_raw_buffer_load_b128(rv, vrow + t * 256, so + (off), AUXKV);
#define SB __builtin_amdgcn_sched_barrier(0);
  u32x4 qf[4], qr, dv[4];
  {
    u32x4 xo, gv[4], uv[4];
    uint2 xr0, xr1;
    SB
    u32x4 wqv[16];
    const auto rq = __builtin_amdgcn_make_buffer_rsrc((void*)(wq + (size_t)wave * 64 * (HQ * DH) + h * DH), 0, 64 * HQ * DH * 2, 0x00020000);
    const int vq = r * 16 * (HQ * DH * 2) + c * 16;
#define LDQ(a) _Pragma("unroll") for (int t = a; t < a + 4; ++t) wqv[t] = __builtin_amdgcn_raw_buffer_load_b128(rq, vq, t * (HQ * DH * 2), 2); SB
    const int ks = has_ffn ? 256 : 64;
    LDQ(0) LDK(Ka, 0, ks) SB LDQ(4) LDV(Va, 0) SB LDQ(8) LDK(Kb, TSTR, 256) SB LDQ(12) LDV(Vb, TSTR) SB
    if (has_ffn) {
      xo = ld16(x + (1 - b) * DM + 8 * lane);
      xr0 = *reinterpret_cast<const uint2*>(x + wave * 64 + 4 * (lane >> 2));
      xr1 = *reinterpret_cast<const uint2*>(x + DM + wave * 64 + 4 * (lane >> 2));
      const size_t fo = (size_t)(wave * 64 + 4 * (lane >> 2)) * FF + unit * 32 + 8 * (lane & 3);
#pragma unroll
      for (int t = 0; t < 4; ++t) gv[t] = ldnt(wg + fo + (size_t)t * FF);
#pragma unroll
      for (int t = 0; t < 4; ++t) uv[t] = ldnt(wu + fo + (size_t)t * FF);
    }
    SB
    float mb, den;
    ln_stats(xv, mb, den);
    const float xn = rbf(__uint_as_float((unsigned)xe << 16) - mb) / den;
    const unsigned X = packbf(xn, xn);
    f32x4 qa = {0.f, 0.f, 0.f, 0.f};
#define QSTEP(t) qa = mfma(diag(dppu<0x150 + t>(X), mask), wqv[t], qa);
    QSTEP(0) QSTEP(1) QSTEP(2) QSTEP(3) QSTEP(4) QSTEP(5) QSTEP(6) QSTEP(7)
    QSTEP(8) QSTEP(9) QSTEP(10) QSTEP(11) QSTEP(12) QSTEP(13) QSTEP(14) QSTEP(15)
#undef QSTEP
    if (r < 2) *reinterpret_cast<f32x4*>(lds + L_QP + wave * 512 + (8 * c + 4 * r) * 4) = qa;
    __syncthreads();
    float q0 = 0.f, q1 = 0.f;
#pragma unroll
    for (int w = 0; w < NWAVE; ++w) {
      const float2 v = *reinterpret_cast<const float2*>(lds + L_QP + w * 512 + lane * 8);
      q0 += v.x; q1 += v.y;
    }
    *reinterpret_cast<unsigned*>(lds + L_QB + wave * 256 + lane * 4) = packbf(q0, q1);
    __builtin_amdgcn_fence(__ATOMIC_RELEASE, "wavefront");
    __builtin_amdgcn_wave_barrier();
    __builtin_amdgcn_fence(__ATOMIC_ACQUIRE, "wavefront");
#pragma unroll
    for (int k = 0; k < 4; ++k) qf[k] = *reinterpret_cast<const u32x4*>(lds + L_QB + wave * 256 + k * 64 + r * 16);
    qr = *reinterpret_cast<const u32x4*>(lds + L_QB + wave * 256 + c * 16);
    if (has_ffn) {
      float mo, deo;
      ln_stats(xo, mo, deo);
      const float m0 = b ? mo : mb, d0 = b ? deo : den, m1 = b ? mb : mo, d1 = b ? den : deo;
      unsigned xp0[2], xp1[2];
      xp0[0] = packbf(rbf(bflo(xr0.x) - m0) / d0, rbf(bfhi(xr0.x) - m0) / d0);
      xp0[1] = packbf(rbf(bflo(xr0.y) - m0) / d0, rbf(bfhi(xr0.y) - m0) / d0);
      xp1[0] = packbf(rbf(bflo(xr1.x) - m1) / d1, rbf(bfhi(xr1.x) - m1) / d1);
      xp1[1] = packbf(rbf(bflo(xr1.y) - m1) / d1, rbf(bfhi(xr1.y) - m1) / d1);
      float f[2][2][8];
#pragma unroll
      for (int k = 0; k < 32; ++k) (&f[0][0][0])[k] = 0.f;
#pragma unroll
      for (int m = 0; m < 2; ++m)
#pragma unroll
        for (int k = 0; k < 4; ++k) {
          const unsigned glo = __builtin_amdgcn_perm(gv[2 * m + 1][k], gv[2 * m][k], 0x05040100u);
          const unsigned ghi = __builtin_amdgcn_perm(gv[2 * m + 1][k], gv[2 * m][k], 0x07060302u);
          const unsigned ulo = __builtin_amdgcn_perm(uv[2 * m + 1][k], uv[2 * m][k], 0x05040100u);
          const unsigned uhi = __builtin_amdgcn_perm(uv[2 * m + 1][k], uv[2 * m][k], 0x07060302u);
          f[0][0][2 * k] = dot2(glo, xp0[m], f[0][0][2 * k]); f[0][0][2 * k + 1] = dot2(ghi, xp0[m], f[0][0][2 * k + 1]);
          f[0][1][2 * k] = dot2(glo, xp1[m], f[0][1][2 * k]); f[0][1][2 * k + 1] = dot2(ghi, xp1[m], f[0][1][2 * k + 1]);
          f[1][0][2 * k] = dot2(ulo, xp0[m], f[1][0][2 * k]); f[1][0][2 * k + 1] = dot2(uhi, xp0[m], f[1][0][2 * k + 1]);
          f[1][1][2 * k] = dot2(ulo, xp1[m], f[1][1][2 * k]); f[1][1][2 * k + 1] = dot2(uhi, xp1[m], f[1][1][2 * k + 1]);
        }
      float gu[2];
#pragma unroll
      for (int m = 0; m < 2; ++m) {
        float a2[2][2];
#pragma unroll
        for (int bb = 0; bb < 2; ++bb)
#pragma unroll
          for (int j2 = 0; j2 < 2; ++j2) {
            const float e0 = swap32_add(f[m][bb][4 * j2 + 0], f[m][bb][4 * j2 + 2]);
            const float e1 = swap32_add(f[m][bb][4 * j2 + 1], f[m][bb][4 * j2 + 3]);
            a2[bb][j2] = swap16_add(e0, e1);
          }
        float a3[2];
#pragma unroll
        for (int j2 = 0; j2 < 2; ++j2) {
          const float keep = (c & 8) ? a2[1][j2] : a2[0][j2], send = (c & 8) ? a2[0][j2] : a2[1][j2];
          a3[j2] = keep + dpp<ROR8>(send);
        }
        const float s0 = dpp<0x124>(a3[1]), s1 = dpp<0x12C>(a3[0]);
        gu[m] = (c & 4) ? a3[1] + s0 : a3[0] + s1;
      }
      *reinterpret_cast<float2*>(lds + L_FP + wave * 512 + lane * 8) = make_float2(gu[0], gu[1]);
      SB
#pragma unroll
      for (int t = 0; t < 4; ++t) dv[t] = ldnt(wd + (size_t)(unit * 32 + (wave >> 2) * 16 + 4 * t + r) * DM + (wave & 3) * 128 + 8 * c);
      SB
    }
  }

  float m = -INFINITY, l = 0.f;
  f32x4 acc = {0.f, 0.f, 0.f, 0.f};
#define STEP(KR, VR, REFILL, off, KS)                                                      \
  {                                                                                      \
    f32x4 S = {0.f, 0.f, 0.f, 0.f};                                                      \
    if (KS == 64) {                                                                      \
      _Pragma("unroll") for (int t = 0; t < 4; ++t) S = mfma(KR[t], qf[t], S);           \
    } else {                                                                             \
      _Pragma("unroll") for (int j = 0; j < 4; ++j)                                      \
        _Pragma("unroll") for (int t = 0; t < 4; ++t) S[t] = dot2(KR[t][j], qr[j], S[t]); \
      _Pragma("unroll") for (int t = 0; t < 4; ++t) S[t] += dpp<ROR8>(S[t]);             \
      _Pragma("unroll") for (int t = 0; t < 4; ++t) S[t] += dpp<ROR4>(S[t]);             \
      _Pragma("unroll") for (int t = 0; t < 4; ++t) S[t] += dpp<ROR2>(S[t]);             \
      _Pragma("unroll") for (int t = 0; t < 4; ++t) S[t] += dpp<ROR1>(S[t]);             \
    }                                                                                    \
    SB                                                                                   \
    if (REFILL) { LDK(KR, off, KS) }                                                     \
    SB                                                                                   \
    float sb[4];                                                                         \
    _Pragma("unroll") for (int k = 0; k < 4; ++k) sb[k] = rbf(rbf(S[k]) * SCALE);        \
    float mx = fmaxf(fmaxf(sb[0], sb[1]), fmaxf(sb[2], sb[3]));                          \
    mx = fmaxf(xrow_max(mx), m);                                                         \
    const float alpha = __builtin_amdgcn_exp2f((m - mx) * LOG2E);                        \
    const float nb = -mx * LOG2E;                                                        \
    m = mx; l *= alpha; acc *= alpha;                                                    \
    _Pragma("unroll") for (int k = 0; k < 4; ++k) {                                      \
      const float p = __builtin_amdgcn_exp2f(fmaf(sb[k], LOG2E, nb));                    \
      l += p;                                                                            \
      const float y = (c & 8) ? p - rbf(p) : p;                                          \
      acc = mfma(diag(packbf(y, y), mask), VR[k], acc);                                  \
    }                                                                                    \
    SB                                                                                   \
    if (REFILL) { LDV(VR, off) }                                                         \
    SB                                                                                   \
  }
#define MAINLOOP(KS)                                                                     \
  {                                                                                      \
    _Pragma("unroll 1") for (int k = 2; k < NT_WAVE; k += 2) {                           \
      so += 2 * TSTR;                                                                    \
      STEP(Ka, Va, true, 0, KS)                                                          \
      STEP(Kb, Vb, true, TSTR, 256)                                                      \
    }                                                                                    \
    STEP(Ka, Va, false, 0, KS)                                                           \
    STEP(Kb, Vb, false, 0, 256)                                                          \
  }
  if (has_ffn) MAINLOOP(256) else MAINLOOP(64)
#undef MAINLOOP
#undef STEP

  {
    l = xrow_sum(l);
    f32x4 o;
#pragma unroll
    for (int k2 = 0; k2 < 4; ++k2) o[k2] = swap32_add(acc[k2], acc[k2]);
    if (r < 2) *reinterpret_cast<f32x4*>(lds + L_OW + wave * 512 + (8 * c + 4 * r) * 4) = o;
    if (lane == 0) *reinterpret_cast<float2*>(lds + L_ML + wave * 8) = make_float2(m, l);
  }
  u32x4 wf[16];
  const auto ro = __builtin_amdgcn_make_buffer_rsrc((void*)(wo + (size_t)(h * DH + 16 * wave) * DM), 0, 16 * DM * 2, 0x00020000);
#pragma unroll
  for (int k2 = 0; k2 < 16; ++k2) wf[k2] = __builtin_amdgcn_raw_buffer_load_b128(ro, r * 4096 + c * 16 + k2 * 256, 0, 2);
  __syncthreads();
    if (has_ffn) {
      float g = 0.f, u = 0.f;
#pragma unroll
      for (int w = 0; w < NWAVE; ++w) {
        const float2 v = *reinterpret_cast<const float2*>(lds + L_FP + w * 512 + lane * 8);
        g += v.x; u += v.y;
      }
      const float hv = rbf(gelu_tanh(rbf(g))) * rbf(u);
      *reinterpret_cast<unsigned*>(lds + L_HX + wave * 256 + (((c >> 3) * 4 + r) * 8 + (c & 3) * 2 + ((c >> 2) & 1)) * 4) = packbf(hv, hv);
    }
    __builtin_amdgcn_fence(__ATOMIC_RELEASE, "wavefront");
    __builtin_amdgcn_wave_barrier();
    __builtin_amdgcn_fence(__ATOMIC_ACQUIRE, "wavefront");
    if (has_ffn) {
      const u32x4 h0 = *reinterpret_cast<const u32x4*>(lds + L_HX + wave * 256 + ((c >> 3) * 4 + r) * 32 + (wave >> 2) * 16);
      f32x4 d = {0.f, 0.f, 0.f, 0.f};
#pragma unroll
      for (int t = 0; t < 4; ++t) d = mfma(diag(h0[t], mask), dv[t], d);
      *reinterpret_cast<f32x4*>(reinterpret_cast<float*>(ws + WS_F) + ((size_t)(r >> 1) * NFP + unit * 2 + (wave >> 2)) * DM + (wave & 3) * 128 + 8 * c + 4 * (r & 1)) = d;
    }
  float Mb = -INFINITY, Lb = 0.f;
  {
    float2 mlw[NWAVE];
#pragma unroll
    for (int w = 0; w < NWAVE; ++w) { mlw[w] = *reinterpret_cast<const float2*>(lds + L_ML + w * 8); Mb = fmaxf(Mb, mlw[w].x); }
    float o = 0.f;
#pragma unroll
    for (int w = 0; w < NWAVE; ++w) {
      const float cw = __builtin_amdgcn_exp2f((mlw[w].x - Mb) * LOG2E);
      Lb = fmaf(cw, mlw[w].y, Lb);
      o = fmaf(cw, *reinterpret_cast<const float*>(lds + L_OW + w * 512 + (wave * 16 + 4 * r + (c & 3)) * 4), o);
    }
    const float y = (c & 8) ? o - rbf(o) : o;
    const unsigned X = packbf(y, y);
    const u32x4 A0 = diag(dppu<0x00>(X), mask), A1 = diag(dppu<0x55>(X), mask);
    const u32x4 A2 = diag(dppu<0xAA>(X), mask), A3 = diag(dppu<0xFF>(X), mask);
#pragma unroll
    for (int sg = 0; sg < 4; ++sg) {
      f32x4 d = mfma(A0, wf[sg], f32x4{0.f, 0.f, 0.f, 0.f});
      d = mfma(A1, wf[4 + sg], d);
      d = mfma(A2, wf[8 + sg], d);
      d = mfma(A3, wf[12 + sg], d);
      *reinterpret_cast<f32x4*>(lds + L_WO + wave * 4096 + (r >> 1) * 2048 + (sg * 128 + 8 * c + 4 * (r & 1)) * 4) = d;
    }
  }
  __syncthreads();
  {
    float y = 0.f;
#pragma unroll
    for (int w = 0; w < NWAVE; ++w)
      y += *reinterpret_cast<const float*>(lds + L_WO + w * 4096 + tid * 4) + *reinterpret_cast<const float*>(lds + L_WO + w * 4096 + 2048 + tid * 4);
    const int slot = b * NSLOT + h * NSPLIT + s;
    reinterpret_cast<float*>(ws + WS_Y)[(size_t)slot * DM + tid] = y;
    if (tid == 0) *reinterpret_cast<float2*>(ws + WS_ML + (size_t)slot * 8) = make_float2(Mb * LOG2E, Lb);
  }
}

__global__ void __launch_bounds__(256)
k_final(const u16* __restrict__ x, const unsigned char* __restrict__ ws, u16* __restrict__ out) {
  __shared__ __attribute__((aligned(16))) float red[4][2][8];
  const int tid = threadIdx.x, lane = tid & 63, wave = tid >> 6;
  const int b = blockIdx.x & 1, cg = blockIdx.x >> 1;
  const int row = tid >> 1, half = tid & 1;
  const f32x4 y = *reinterpret_cast<const f32x4*>(ws + WS_Y + (((size_t)b * NSLOT + row) * DM + cg * 8 + half * 4) * 4);
  const f32x4* mlp = reinterpret_cast<const f32x4*>(ws + WS_ML + ((size_t)b * NSLOT + (row & ~3)) * 8);
  const f32x4 a = mlp[0], c4 = mlp[1];
  const int frow = row < NFP ? row : 0;
  f32x4 sf = *reinterpret_cast<const f32x4*>(ws + WS_F + (((size_t)b * NFP + frow) * DM + cg * 8 + half * 4) * 4);
  const u16 xo = x[b * DM + cg * 8 + (tid & 7)];
  __builtin_amdgcn_sched_barrier(0);
  if (row >= NFP) sf = f32x4{0.f, 0.f, 0.f, 0.f};
  const float M = fmaxf(fmaxf(a[0], a[2]), fmaxf(c4[0], c4[2]));
  const float e0 = __builtin_amdgcn_exp2f(a[0] - M), e1 = __builtin_amdgcn_exp2f(a[2] - M);
  const float e2 = __builtin_amdgcn_exp2f(c4[0] - M), e3 = __builtin_amdgcn_exp2f(c4[2] - M);
  const float L = fmaf(e0, a[1], fmaf(e1, a[3], fmaf(e2, c4[1], e3 * c4[3])));
  const int sp = row & 3;
  const float e = sp == 0 ? e0 : sp == 1 ? e1 : sp == 2 ? e2 : e3;
  const f32x4 sa = y * (e / L);
  float v[8] = {sa[0], sa[1], sa[2], sa[3], sf[0], sf[1], sf[2], sf[3]};
  float u[4], w2[2];
#pragma unroll
  for (int k = 0; k < 4; ++k) u[k] = swap32_add(v[k], v[4 + k]);
#pragma unroll
  for (int k = 0; k < 2; ++k) w2[k] = swap16_add(u[k], u[2 + k]);
#pragma unroll
  for (int k = 0; k < 2; ++k) { float t = w2[k]; t += dpp<XOR2>(t); t += dpp<ROR4>(t); t += dpp<ROR8>(t); w2[k] = t; }
  if ((lane & 14) == 0)
    *reinterpret_cast<float2*>(&red[wave][lane >> 5][(lane & 1) * 4 + ((lane >> 4) & 1) * 2]) = make_float2(w2[0], w2[1]);
  __syncthreads();
  if (tid < 8) {
    float s = (red[0][0][tid] + red[1][0][tid]) + (red[2][0][tid] + red[3][0][tid]);
    float f = (red[0][1][tid] + red[1][1][tid]) + (red[2][1][tid] + red[3][1][tid]);
    s = rbf(s); f = rbf(f);
    const float g = rbf(f / (1.f + __expf(-f)));
    const float o = __uint_as_float((unsigned)xo << 16) + rbf(s * g);
    out[b * DM + cg * 8 + tid] = __builtin_bit_cast(u16, (__bf16)o);
  }
}

extern "C" void kernel_launch(void* const* d_in, const int* in_sizes, int n_in, void* d_out, int out_size, void* d_ws,
                              size_t ws_size, hipStream_t stream) {
  const u16* x = (const u16*)d_in[0];
  const u16* kc = (const u16*)d_in[1];
  const u16* vc = (const u16*)d_in[2];
  const u16* wq = (const u16*)d_in[3];
  const u16* wo = (const u16*)d_in[4];
  const u16* wg = (const u16*)d_in[6];
  const u16* wu = (const u16*)d_in[7];
  const u16* wd = (const u16*)d_in[8];
  k_main<<<256, 512, 0, stream>>>(x, kc, vc, wq, wo, wg, wu, wd, (unsigned char*)d_ws);
  k_final<<<128, 256, 0, stream>>>(x, (const unsigned char*)d_ws, (u16*)d_out);
}
```
